```python
import torch

def _softmax(x, axis=-1):
    return torch.softmax(x, dim=axis)
def _gelu(x):
    return torch.nn.functional.gelu(x, approximate="tanh")
def _silu(x):
    return torch.nn.functional.silu(x)
def _sigmoid(x):
    return torch.sigmoid(x)
def _layernorm(x, axis=-1, eps=1e-5):
    m = x.mean(dim=axis, keepdim=True)
    v = x.var(dim=axis, keepdim=True, unbiased=False)
    return (x - m) / torch.sqrt(v + eps)
def _scatter_add(tgt, idx, src, axis=0):
    return tgt.clone().index_add_(axis, idx, src)
def _segment_sum(src, seg, n):
    out = torch.zeros((n,) + src.shape[1:], dtype=src.dtype)
    return out.index_add_(0, seg, src)
def _pad(x, pw):
    flat = tuple(v for pair in reversed(pw) for v in pair)
    return torch.nn.functional.pad(x, flat)

kernel_name = 'sequential_preLN__spectral_fnet__moe_arch_mod__prompt'

def setup_inputs():

    _w = (lambda a: (lambda t: t.to(torch.bfloat16) if t.is_floating_point() else t)(torch.as_tensor(a)))
    def _A_setup():
        B, S, D = 1, 2048, 512
        ns = torch.arange(S)[:, None] * torch.arange(S)[None, :]
        nd = torch.arange(D)[:, None] * torch.arange(D)[None, :]
        Ws_re = torch.cos(-2.0 * torch.pi * ns / S).to(float)
        Ws_im = torch.sin(-2.0 * torch.pi * ns / S).to(float)
        Wd_re = torch.cos(-2.0 * torch.pi * nd / D).to(float)
        Wd_im = torch.sin(-2.0 * torch.pi * nd / D).to(float)
        return {
            "x": _w((lambda *s: torch.randn(*s, dtype=torch.bfloat16) * 0.05)(B, S, D)),
            "dft_seq_re": _w(Ws_re),
            "dft_seq_im": _w(Ws_im),
            "dft_hid_re": _w(Wd_re),
            "dft_hid_im": _w(Wd_im),
        }
    _A_in = _A_setup()

    def _B_setup():
        B, S, D, F, C = 1, 2048, 512, 1792, 1
        return {
            "x": _w((lambda *s: torch.randn(*s, dtype=torch.bfloat16) * 0.05)(B, S, D)),
            "w_r": _w((lambda *s: torch.randn(*s, dtype=torch.bfloat16) * 1.6)(D)),
            "w_gate": _w((lambda *s: torch.randn(*s, dtype=torch.bfloat16) * 0.4)(D, F)),
            "w_up": _w((lambda *s: torch.randn(*s, dtype=torch.bfloat16) * 0.4)(D, F)),
            "w_down": _w((lambda *s: torch.randn(*s, dtype=torch.bfloat16) * 0.4)(F, D)),
        }
    _B_in = _B_setup()

    return {
        "A_x": _A_in["x"],
        "A_dft_seq_re": _A_in["dft_seq_re"],
        "A_dft_seq_im": _A_in["dft_seq_im"],
        "A_dft_hid_re": _A_in["dft_hid_re"],
        "A_dft_hid_im": _A_in["dft_hid_im"],
        "B_x": _B_in["x"],
        "B_w_r": _B_in["w_r"],
        "B_w_gate": _B_in["w_gate"],
        "B_w_up": _B_in["w_up"],
        "B_w_down": _B_in["w_down"],
    }

def reference(A_x, A_dft_seq_re, A_dft_seq_im, A_dft_hid_re, A_dft_hid_im, B_x, B_w_r, B_w_gate, B_w_up, B_w_down):
    def _A_ref(x, dft_seq_re, dft_seq_im, dft_hid_re, dft_hid_im):
        a = torch.einsum("bsd,ed->bse", x, dft_hid_re)
        b = torch.einsum("bsd,ed->bse", x, dft_hid_im)
        y = torch.einsum("ts,bse->bte", dft_seq_re, a) - torch.einsum("ts,bse->bte", dft_seq_im, b)
        return y

    def _B_ref(x, w_r, w_gate, w_up, w_down):
        B, S, D = x.shape
        C = 1
        r = x @ w_r
        idx = torch.argsort(-r, axis=-1)[:, :C]
        idx_e = (lambda x, n, axis: torch.repeat_interleave(x, n, dim=axis))(idx.reshape(B, C, 1), D, axis=2)
        x_sel = (lambda x, i, axis: torch.take_along_dim(x, i, dim=axis))(x, idx_e, axis=1)
        g = _silu(x_sel @ w_gate)
        u = x_sel @ w_up
        y = (g * u) @ w_down
        r_sel = _sigmoid((lambda x, i, axis: torch.take_along_dim(x, i, dim=axis))(r, idx, axis=1))
        y = y * r_sel.reshape(B, C, 1)
        b_off = (torch.arange(B) * S).reshape(B, 1)
        flat_idx = (idx + b_off).reshape(B * C)
        zero = (x * 0.0).reshape(B * S, D)
        upd = _scatter_add(zero, flat_idx, y.reshape(B * C, D), axis=0)
        return x + upd.reshape(B, S, D)

    x = A_x
    h = x + _A_ref(_layernorm(x, axis=-1), A_dft_seq_re, A_dft_seq_im, A_dft_hid_re, A_dft_hid_im)
    h2 = h + _B_ref(_layernorm(h, axis=-1), B_w_r, B_w_gate, B_w_up, B_w_down)
    return h2
```

```cpp
#include <hip/hip_runtime.h>
#include <stdint.h>

typedef unsigned short bf16_t;
typedef short bf16x8 __attribute__((ext_vector_type(8)));
typedef float f32x4 __attribute__((ext_vector_type(4)));
typedef unsigned u32x4 __attribute__((ext_vector_type(4)));
typedef unsigned u32x2 __attribute__((ext_vector_type(2)));

constexpr int S_ = 2048, D_ = 512, F_ = 1792;

__device__ __forceinline__ float bf2f(unsigned short b) { return __uint_as_float(((unsigned)b) << 16); }
__device__ __forceinline__ float bflo(unsigned w) { return __uint_as_float(w << 16); }
__device__ __forceinline__ float bfhi(unsigned w) { return __uint_as_float(w & 0xffff0000u); }
typedef __bf16 bf16x2_t __attribute__((ext_vector_type(2)));
__device__ __forceinline__ unsigned pack2(float lo, float hi) {
    bf16x2_t v; v.x = (__bf16)lo; v.y = (__bf16)hi;
    return __builtin_bit_cast(unsigned, v);
}
__device__ __forceinline__ float rbf(float x) {
    __bf16 b = (__bf16)x; return (float)b;
}

template <int CTRL>
__device__ __forceinline__ float dpp_add(float v) {
    int o = __builtin_amdgcn_update_dpp(0, __float_as_int(v), CTRL, 0xf, 0xf, true);
    return v + __int_as_float(o);
}
__device__ __forceinline__ float row16_sum(float v) {
    v = dpp_add<0xB1>(v);
    v = dpp_add<0x4E>(v);
    v = dpp_add<0x141>(v);
    v = dpp_add<0x140>(v);
    return v;
}
__device__ __forceinline__ float wave_sum(float v) {
    v = row16_sum(v);
    float a = __int_as_float(__builtin_amdgcn_readlane(__float_as_int(v), 0));
    float b = __int_as_float(__builtin_amdgcn_readlane(__float_as_int(v), 16));
    float c = __int_as_float(__builtin_amdgcn_readlane(__float_as_int(v), 32));
    float d = __int_as_float(__builtin_amdgcn_readlane(__float_as_int(v), 48));
    return (a + b) + (c + d);
}

__global__ __launch_bounds__(256, 1) void k1_ln_gemm1(const bf16_t* __restrict__ x, const bf16_t* __restrict__ hr,
                                                      const bf16_t* __restrict__ hi, bf16_t* __restrict__ abT,
                                                      unsigned* __restrict__ counter) {
    __shared__ __attribute__((aligned(16))) char smem[64 * 1024];
    const int tid = threadIdx.x, w = tid >> 6, lane = tid & 63, fr = lane & 15, fq = lane >> 4;
    const int b = blockIdx.x;
    const int et = b & 7, st = b >> 3;
    const int s0 = st * 64, e0 = et * 128;
    if (b == 0 && tid == 0) counter[0] = 0u;

    u32x4 xv[4][4];
#pragma unroll
    for (int g = 0; g < 4; ++g)
#pragma unroll
        for (int i = 0; i < 4; ++i)
            xv[g][i] = *(const u32x4*)(x + (size_t)(s0 + w * 16 + g * 4 + fq) * D_ + (i * 16 + fr) * 8);
    const bf16_t* hsrc = (et < 4) ? (hr + (size_t)(e0)*D_) : (hi + (size_t)(e0 - 512) * D_);
    bf16x8 hb[2][16];
#pragma unroll
    for (int kk = 0; kk < 16; ++kk)
#pragma unroll
        for (int n = 0; n < 2; ++n)
            hb[n][kk] = *(const bf16x8*)(hsrc + (size_t)(w * 32 + n * 16 + fr) * D_ + kk * 32 + fq * 8);

#pragma unroll
    for (int g = 0; g < 4; ++g) {
        float v[32];
#pragma unroll
        for (int i = 0; i < 4; ++i)
#pragma unroll
            for (int j = 0; j < 4; ++j) { v[i * 8 + j * 2] = bflo(xv[g][i][j]); v[i * 8 + j * 2 + 1] = bfhi(xv[g][i][j]); }
        float s = 0.f;
#pragma unroll
        for (int j = 0; j < 32; ++j) s += v[j];
        s = row16_sum(s);
        const float mean = s * (1.0f / D_);
        float q = 0.f;
#pragma unroll
        for (int j = 0; j < 32; ++j) { float d = v[j] - mean; q += d * d; }
        q = row16_sum(q);
        const float mb = rbf(mean);
        const float vb = rbf(q * (1.0f / D_));
        const float sd = rbf(__builtin_sqrtf(rbf(vb + 1e-5f)));
        const float inv = 1.0f / sd;
        const int r = w * 16 + g * 4 + fq;
        const int key = ((r >> 3) & 3) * 4 + (r & 3);
#pragma unroll
        for (int i = 0; i < 4; ++i) {
            u32x4 o;
#pragma unroll
            for (int j = 0; j < 4; ++j) {
                float a0 = rbf(v[i * 8 + j * 2] - mb), a1 = rbf(v[i * 8 + j * 2 + 1] - mb);
                o[j] = pack2(a0 / sd, a1 / sd);
            }
            const int c = (i * 16 + fr) ^ key;
            *(u32x4*)(smem + r * 1024 + c * 16) = o;
        }
        (void)inv;
    }
    __syncthreads();

    f32x4 acc[4][2];
#pragma unroll
    for (int m = 0; m < 4; ++m)
#pragma unroll
        for (int n = 0; n < 2; ++n) acc[m][n] = (f32x4){0.f, 0.f, 0.f, 0.f};
#pragma unroll
    for (int kk = 0; kk < 16; ++kk) {
        bf16x8 a[4];
#pragma unroll
        for (int m = 0; m < 4; ++m) {
            const int r = (fr >> 2) * 8 + (m & 1) * 4 + (fr & 3) + (m >> 1) * 32;
            const int c = (kk * 4 + fq) ^ fr;
            a[m] = *(const bf16x8*)(smem + r * 1024 + c * 16);
        }
#pragma unroll
        for (int m = 0; m < 4; ++m)
#pragma unroll
            for (int n = 0; n < 2; ++n)
                acc[m][n] = __builtin_amdgcn_mfma_f32_16x16x32_bf16(a[m], hb[n][kk], acc[m][n], 0, 0, 0);
    }
    const float sgn = (et < 4) ? 1.0f : -1.0f;
#pragma unroll
    for (int n = 0; n < 2; ++n)
#pragma unroll
        for (int p = 0; p < 2; ++p) {
            u32x4 o;
            o[0] = pack2(sgn * acc[2 * p][n][0], sgn * acc[2 * p][n][1]);
            o[1] = pack2(sgn * acc[2 * p][n][2], sgn * acc[2 * p][n][3]);
            o[2] = pack2(sgn * acc[2 * p + 1][n][0], sgn * acc[2 * p + 1][n][1]);
            o[3] = pack2(sgn * acc[2 * p + 1][n][2], sgn * acc[2 * p + 1][n][3]);
            *(u32x4*)(abT + (size_t)(e0 + w * 32 + n * 16 + fr) * S_ + s0 + p * 32 + fq * 8) = o;
        }
}

constexpr int K2_STAGES = 4;
constexpr int K2_STAGE_B = 32 * 1024;
__global__ __launch_bounds__(256, 1) void k2_gemm2(const bf16_t* __restrict__ cr, const bf16_t* __restrict__ ci,
                                                   const bf16_t* __restrict__ abT, float* __restrict__ part) {
    extern __shared__ __attribute__((aligned(16))) char smem[];
    const int tid = threadIdx.x, w = tid >> 6, lane = tid & 63, fr = lane & 15, fq = lane >> 4;
    const int b = blockIdx.x, xcd = b & 7, j = b >> 3;
    const int ks = xcd >> 1, mt = (xcd & 1) * 8 + (j >> 2), nt = j & 3;
    const int t0 = mt * 128, e0 = nt * 128;
    const bf16_t* Cm = (ks < 2 ? cr : ci) + (size_t)t0 * S_ + (ks & 1) * 1024;
    const bf16_t* Bm = abT + (size_t)((ks >> 1) * 512 + e0) * S_ + (ks & 1) * 1024;
    const int wr = w >> 1, wc = w & 1;

    const int srow = lane >> 3, sslot = lane & 7;
    const int schunk = sslot ^ (srow & 7);
    const bf16_t* gA = Cm + (size_t)(w * 8 + srow) * S_ + schunk * 8;
    const bf16_t* gB = Bm + (size_t)(w * 8 + srow) * S_ + schunk * 8;
    auto stage = [&](int kt, int buf) {
        char* base = smem + buf * K2_STAGE_B + w * 1024;
#pragma unroll
        for (int i = 0; i < 4; ++i) {
            __builtin_amdgcn_global_load_lds((const unsigned*)(gA + (size_t)i * 32 * S_ + kt * 64), (unsigned*)(base + i * 4096), 16, 0, 0);
            __builtin_amdgcn_global_load_lds((const unsigned*)(gB + (size_t)i * 32 * S_ + kt * 64), (unsigned*)(base + 16384 + i * 4096), 16, 0, 0);
        }
    };
    f32x4 acc[4][4];
#pragma unroll
    for (int m = 0; m < 4; ++m)
#pragma unroll
        for (int n = 0; n < 4; ++n) acc[m][n] = (f32x4){0.f, 0.f, 0.f, 0.f};

    constexpr int NT = 16;
    stage(0, 0); stage(1, 1); stage(2, 2);
    const int aoff = (wr * 64 + fr) * 128, boff = 16384 + (wc * 64 + fr) * 128;
    for (int t = 0; t < NT; ++t) {
        if (t < NT - 2) asm volatile("s_waitcnt vmcnt(16)" ::: "memory");
        else if (t == NT - 2) asm volatile("s_waitcnt vmcnt(8)" ::: "memory");
        else asm volatile("s_waitcnt vmcnt(0)" ::: "memory");
        asm volatile("s_waitcnt lgkmcnt(0)" ::: "memory");
        __builtin_amdgcn_s_barrier();
        asm volatile("" ::: "memory");
        if (t + 3 < NT) stage(t + 3, (t + 3) & 3);
        const char* sb = smem + (t & 3) * K2_STAGE_B;
#pragma unroll
        for (int kk = 0; kk < 2; ++kk) {
            bf16x8 a[4], bb[4];
            const int c = ((kk * 4 + fq) ^ (fr & 7)) * 16;
#pragma unroll
            for (int m = 0; m < 4; ++m) a[m] = *(const bf16x8*)(sb + aoff + m * 2048 + c);
#pragma unroll
            for (int n = 0; n < 4; ++n) bb[n] = *(const bf16x8*)(sb + boff + n * 2048 + c);
#pragma unroll
            for (int m = 0; m < 4; ++m)
#pragma unroll
                for (int n = 0; n < 4; ++n)
                    acc[m][n] = __builtin_amdgcn_mfma_f32_16x16x32_bf16(bb[n], a[m], acc[m][n], 0, 0, 0);
        }
    }
    float* P = part + (size_t)ks * S_ * D_;
#pragma unroll
    for (int m = 0; m < 4; ++m)
#pragma unroll
        for (int n = 0; n < 4; ++n)
            *(f32x4*)(P + (size_t)(t0 + wr * 64 + m * 16 + fr) * D_ + e0 + wc * 64 + n * 16 + fq * 4) = acc[m][n];
}

__global__ __launch_bounds__(512, 1) void k3_reduce_ln(const float* __restrict__ part, const bf16_t* __restrict__ x,
                                                       const bf16_t* __restrict__ wr_, bf16_t* __restrict__ out,
                                                       float* __restrict__ cand_r, int* __restrict__ cand_i,
                                                       bf16_t* __restrict__ cand_h, bf16_t* __restrict__ cand_hn) {
    __shared__ float s_r[8];
    const int tid = threadIdx.x, w = tid >> 6, lane = tid & 63;
    const int row = blockIdx.x * 8 + w;
    const size_t o = (size_t)row * D_ + lane * 8;
    f32x4 p[4][2];
#pragma unroll
    for (int k = 0; k < 4; ++k) {
        p[k][0] = *(const f32x4*)(part + (size_t)k * S_ * D_ + o);
        p[k][1] = *(const f32x4*)(part + (size_t)k * S_ * D_ + o + 4);
    }
    const u32x4 xv = *(const u32x4*)(x + o);
    const u32x4 wv = *(const u32x4*)(wr_ + lane * 8);
    float h[8];
#pragma unroll
    for (int j = 0; j < 8; ++j) {
        const float re = rbf(p[0][j >> 2][j & 3] + p[1][j >> 2][j & 3]);
        const float im = rbf(p[2][j >> 2][j & 3] + p[3][j >> 2][j & 3]);
        const float y = rbf(re + im);
        const float xx = (j & 1) ? bfhi(xv[j >> 1]) : bflo(xv[j >> 1]);
        h[j] = rbf(xx + y);
    }
    float s = 0.f;
#pragma unroll
    for (int j = 0; j < 8; ++j) s += h[j];
    const float mean = wave_sum(s) * (1.0f / D_);
    float q = 0.f;
#pragma unroll
    for (int j = 0; j < 8; ++j) { float d = h[j] - mean; q += d * d; }
    q = wave_sum(q);
    const float mb = rbf(mean);
    const float vb = rbf(q * (1.0f / D_));
    const float sd = rbf(__builtin_sqrtf(rbf(vb + 1e-5f)));
    float hn[8];
    float rr = 0.f;
#pragma unroll
    for (int j = 0; j < 8; ++j) {
        hn[j] = rbf(rbf(h[j] - mb) / sd);
        const float ww = (j & 1) ? bfhi(wv[j >> 1]) : bflo(wv[j >> 1]);
        rr += hn[j] * ww;
    }
    rr = rbf(wave_sum(rr));
    u32x4 ov;
#pragma unroll
    for (int j = 0; j < 4; ++j) ov[j] = pack2(h[2 * j] + hn[2 * j], h[2 * j + 1] + hn[2 * j + 1]);
    *(u32x4*)(out + o) = ov;
    if (lane == 0) s_r[w] = rr;
    __syncthreads();
    int best = 0; float br = s_r[0];
#pragma unroll
    for (int k = 1; k < 8; ++k) { const float v = s_r[k]; if (v > br) { br = v; best = k; } }
    if (w == best) {
        u32x4 a, c;
#pragma unroll
        for (int j = 0; j < 4; ++j) { a[j] = pack2(h[2 * j], h[2 * j + 1]); c[j] = pack2(hn[2 * j], hn[2 * j + 1]); }
        *(u32x4*)(cand_h + (size_t)blockIdx.x * D_ + lane * 8) = a;
        *(u32x4*)(cand_hn + (size_t)blockIdx.x * D_ + lane * 8) = c;
        if (lane == 0) { cand_r[blockIdx.x] = br; cand_i[blockIdx.x] = row; }
    }
}

constexpr int K4_G = 56;
__global__ __launch_bounds__(256, 1) void k4_ffn(const float* __restrict__ cand_r, const int* __restrict__ cand_i,
                                                 const bf16_t* __restrict__ cand_h, const bf16_t* __restrict__ cand_hn,
                                                 const bf16_t* __restrict__ wg, const bf16_t* __restrict__ wu,
                                                 const bf16_t* __restrict__ wd, float* ypart, unsigned* counter,
                                                 bf16_t* __restrict__ out) {
    __shared__ float s_red[4][16][4];
    __shared__ float s_best[4]; __shared__ int s_bi[4];
    __shared__ float s_hid[32];
    __shared__ float s_y[4][512];
    __shared__ int s_last;
    const int tid = threadIdx.x, w = tid >> 6, lane = tid & 63;
    const int f0 = blockIdx.x * 32;
    const int c = lane & 3, rl = lane >> 2;
    u32x4 g[8], u[8], dn[8];
#pragma unroll
    for (int i = 0; i < 8; ++i) {
        const int d = (i * 4 + w) * 16 + rl;
        g[i] = *(const u32x4*)(wg + (size_t)d * F_ + f0 + c * 8);
        u[i] = *(const u32x4*)(wu + (size_t)d * F_ + f0 + c * 8);
    }
#pragma unroll
    for (int i = 0; i < 8; ++i) dn[i] = *(const u32x4*)(wd + (size_t)(f0 + i * 4 + w) * D_ + lane * 8);

    float br = cand_r[tid]; int bi = tid;
#pragma unroll
    for (int o = 1; o < 64; o <<= 1) {
        const float vr = __shfl_xor(br, o); const int vi = __shfl_xor(bi, o);
        if (vr > br || (vr == br && vi < bi)) { br = vr; bi = vi; }
    }
    if (lane == 0) { s_best[w] = br; s_bi[w] = bi; }
    __syncthreads();
    br = s_best[0]; bi = s_bi[0];
#pragma unroll
    for (int k = 1; k < 4; ++k) { if (s_best[k] > br) { br = s_best[k]; bi = s_bi[k]; } }
    bi = bi & 255;
    float ag[8], au[8];
#pragma unroll
    for (int j = 0; j < 8; ++j) { ag[j] = 0.f; au[j] = 0.f; }
#pragma unroll
    for (int i = 0; i < 8; ++i) {
        const int d = (i * 4 + w) * 16 + rl;
        const float xv = bf2f(cand_hn[(size_t)bi * D_ + d]);
#pragma unroll
        for (int j = 0; j < 4; ++j) {
            ag[2 * j] += xv * bflo(g[i][j]); ag[2 * j + 1] += xv * bfhi(g[i][j]);
            au[2 * j] += xv * bflo(u[i][j]); au[2 * j + 1] += xv * bfhi(u[i][j]);
        }
    }
#pragma unroll
    for (int o = 4; o < 64; o <<= 1)
#pragma unroll
        for (int j = 0; j < 8; ++j) { ag[j] += __shfl_xor(ag[j], o); au[j] += __shfl_xor(au[j], o); }
    if (lane < 4) {
#pragma unroll
        for (int j = 0; j < 8; ++j) { s_red[w][j][c] = ag[j]; s_red[w][8 + j][c] = au[j]; }
    }
    __syncthreads();
    if (tid < 32) {
        const int cc = tid >> 3, j = tid & 7;
        const float gs = (s_red[0][j][cc] + s_red[1][j][cc]) + (s_red[2][j][cc] + s_red[3][j][cc]);
        const float us = (s_red[0][8 + j][cc] + s_red[1][8 + j][cc]) + (s_red[2][8 + j][cc] + s_red[3][8 + j][cc]);
        const float gb = rbf(gs);
        const float sg = rbf(gb / (1.0f + __expf(-gb)));
        s_hid[tid] = rbf(sg * rbf(us));
    }
    __syncthreads();
    float ay[8];
#pragma unroll
    for (int j = 0; j < 8; ++j) ay[j] = 0.f;
#pragma unroll
    for (int i = 0; i < 8; ++i) {
        const float hv = s_hid[i * 4 + w];
#pragma unroll
        for (int j = 0; j < 4; ++j) { ay[2 * j] += hv * bflo(dn[i][j]); ay[2 * j + 1] += hv * bfhi(dn[i][j]); }
    }
#pragma unroll
    for (int j = 0; j < 8; ++j) s_y[w][lane * 8 + j] = ay[j];
    __syncthreads();
    {
        const int c0 = tid * 2;
        const float y0 = (s_y[0][c0] + s_y[1][c0]) + (s_y[2][c0] + s_y[3][c0]);
        const float y1 = (s_y[0][c0 + 1] + s_y[1][c0 + 1]) + (s_y[2][c0 + 1] + s_y[3][c0 + 1]);
        unsigned long long pk = ((unsigned long long)__float_as_uint(y1) << 32) | __float_as_uint(y0);
        __hip_atomic_store((unsigned long long*)(ypart + (size_t)blockIdx.x * D_ + c0), pk, __ATOMIC_RELAXED, __HIP_MEMORY_SCOPE_AGENT);
    }
    asm volatile("s_waitcnt vmcnt(0)" ::: "memory");
    __syncthreads();
    if (tid == 0) {
        const unsigned old = __hip_atomic_fetch_add(counter, 1u, __ATOMIC_RELAXED, __HIP_MEMORY_SCOPE_AGENT);
        s_last = (old == (unsigned)(K4_G - 1));
        if (old == (unsigned)(K4_G - 1)) {
            __builtin_amdgcn_fence(__ATOMIC_ACQUIRE, "agent");
            asm volatile("s_waitcnt vmcnt(0)" ::: "memory");
        }
    }
    __syncthreads();
    if (!s_last) return;
    {
        const int c0 = tid * 2;
        float y0 = 0.f, y1 = 0.f;
        unsigned long long v[K4_G];
#pragma unroll
        for (int k = 0; k < K4_G; ++k)
            v[k] = __hip_atomic_load((unsigned long long*)(ypart + (size_t)k * D_ + c0), __ATOMIC_RELAXED, __HIP_MEMORY_SCOPE_AGENT);
#pragma unroll
        for (int k = 0; k < K4_G; ++k) { y0 += __uint_as_float((unsigned)v[k]); y1 += __uint_as_float((unsigned)(v[k] >> 32)); }
        const float rs = rbf(1.0f / (1.0f + __expf(-br)));
        const int row = cand_i[bi];
        const unsigned hw = *(const unsigned*)(cand_h + (size_t)bi * D_ + c0);
        const unsigned nw = *(const unsigned*)(cand_hn + (size_t)bi * D_ + c0);
        const float u0 = rbf(rbf(y0) * rs), u1 = rbf(rbf(y1) * rs);
        const float o0 = bflo(hw) + rbf(bflo(nw) + u0), o1 = bfhi(hw) + rbf(bfhi(nw) + u1);
        *(unsigned*)(out + (size_t)(row & (S_ - 1)) * D_ + c0) = pack2(o0, o1);
    }
}

extern "C" void kernel_launch(void* const* d_in, const int* in_sizes, int n_in, void* d_out, int out_size, void* d_ws,
                              size_t ws_size, hipStream_t stream) {
    const bf16_t* x = (const bf16_t*)d_in[0];
    const bf16_t* cr = (const bf16_t*)d_in[1];
    const bf16_t* ci = (const bf16_t*)d_in[2];
    const bf16_t* hr = (const bf16_t*)d_in[3];
    const bf16_t* hi = (const bf16_t*)d_in[4];
    const bf16_t* wr = (const bf16_t*)d_in[6];
    const bf16_t* wg = (const bf16_t*)d_in[7];
    const bf16_t* wu = (const bf16_t*)d_in[8];
    const bf16_t* wd = (const bf16_t*)d_in[9];
    bf16_t* out = (bf16_t*)d_out;
    char* ws = (char*)d_ws;
    bf16_t* abT = (bf16_t*)(ws);
    float* part = (float*)(ws + (4u << 20));
    char* small = ws + (20u << 20);
    unsigned* counter = (unsigned*)(small);
    float* cand_r = (float*)(small + 1024);
    int* cand_i = (int*)(small + 2048);
    bf16_t* cand_h = (bf16_t*)(small + 4096);
    bf16_t* cand_hn = (bf16_t*)(small + 4096 + 262144);
    float* ypart = (float*)(small + 4096 + 2 * 262144);

    hipFuncSetAttribute((const void*)k2_gemm2, hipFuncAttributeMaxDynamicSharedMemorySize, K2_STAGES * K2_STAGE_B);
    k1_ln_gemm1<<<256, 256, 0, stream>>>(x, hr, hi, abT, counter);
    k2_gemm2<<<256, 256, K2_STAGES * K2_STAGE_B, stream>>>(cr, ci, abT, part);
    k3_reduce_ln<<<256, 512, 0, stream>>>(part, x, wr, out, cand_r, cand_i, cand_h, cand_hn);
    k4_ffn<<<K4_G, 256, 0, stream>>>(cand_r, cand_i, cand_h, cand_hn, wg, wu, wd, ypart, counter, out);
}
```

```cpp
#include <hip/hip_runtime.h>
#include <stdint.h>

typedef unsigned short bf16_t;
typedef short bf16x8 __attribute__((ext_vector_type(8)));
typedef float f32x4 __attribute__((ext_vector_type(4)));
typedef unsigned u32x4 __attribute__((ext_vector_type(4)));
typedef unsigned u32x2 __attribute__((ext_vector_type(2)));
typedef unsigned long long u64;

#define PREFETCH_C 1
#define K1_SLEEP 10
#define PREFETCH_W 1
#define K2_NB 4

#define STAMP(k, i)
#define STAMPT(k, i, T)
#define DBGARG
#define DBGPASS

constexpr int S_ = 2048, D_ = 512, F_ = 1792;
constexpr int FG = 56;

__device__ __forceinline__ float bf2f(unsigned short b) { return __uint_as_float(((unsigned)b) << 16); }
__device__ __forceinline__ float bflo(unsigned w) { return __uint_as_float(w << 16); }
__device__ __forceinline__ float bfhi(unsigned w) { return __uint_as_float(w & 0xffff0000u); }
typedef __bf16 bf16x2_t __attribute__((ext_vector_type(2)));
__device__ __forceinline__ unsigned pack2(float lo, float hi) {
    bf16x2_t v; v.x = (__bf16)lo; v.y = (__bf16)hi;
    return __builtin_bit_cast(unsigned, v);
}
__device__ __forceinline__ float rbf(float x) { __bf16 b = (__bf16)x; return (float)b; }

template <int CTRL>
__device__ __forceinline__ float dpp_add(float v) {
    int o = __builtin_amdgcn_update_dpp(0, __float_as_int(v), CTRL, 0xf, 0xf, true);
    return v + __int_as_float(o);
}
__device__ __forceinline__ float row16_sum(float v) {
    v = dpp_add<0xB1>(v); v = dpp_add<0x4E>(v); v = dpp_add<0x141>(v); v = dpp_add<0x140>(v);
    return v;
}
__device__ __forceinline__ float rows_total(float v) {
    float a = __int_as_float(__builtin_amdgcn_readlane(__float_as_int(v), 0));
    float b = __int_as_float(__builtin_amdgcn_readlane(__float_as_int(v), 16));
    float c = __int_as_float(__builtin_amdgcn_readlane(__float_as_int(v), 32));
    float d = __int_as_float(__builtin_amdgcn_readlane(__float_as_int(v), 48));
    return (a + b) + (c + d);
}
__device__ __forceinline__ float wave_sum(float v) { return rows_total(row16_sum(v)); }
template <int CTRL>
__device__ __forceinline__ unsigned dpp_umax(unsigned v) {
    unsigned o = (unsigned)__builtin_amdgcn_update_dpp(0, (int)v, CTRL, 0xf, 0xf, true);
    return v > o ? v : o;
}
__device__ __forceinline__ unsigned wave_umax(unsigned v) {
    v = dpp_umax<0xB1>(v); v = dpp_umax<0x4E>(v); v = dpp_umax<0x141>(v); v = dpp_umax<0x140>(v);
    unsigned a = __builtin_amdgcn_readlane(v, 0), b = __builtin_amdgcn_readlane(v, 16);
    unsigned c = __builtin_amdgcn_readlane(v, 32), d = __builtin_amdgcn_readlane(v, 48);
    a = a > b ? a : b; c = c > d ? c : d;
    return a > c ? a : c;
}

#define WAIT_V(n) asm volatile("s_waitcnt vmcnt(%0)" ::"n"(n) : "memory")
#define WAIT_L0() asm volatile("s_waitcnt lgkmcnt(0)" ::: "memory")
#define BAR() do { asm volatile("" ::: "memory"); __builtin_amdgcn_s_barrier(); asm volatile("" ::: "memory"); } while (0)
#define GLDS(src, dst) __builtin_amdgcn_global_load_lds((const unsigned*)(src), (unsigned*)(dst), 16, 0, 0)
#define SCHED0() __builtin_amdgcn_sched_barrier(0)

__global__ __launch_bounds__(768, 1) void k1_ln_gemm1(const bf16_t* __restrict__ x, const bf16_t* __restrict__ hr,
                                                       const bf16_t* __restrict__ hi, const bf16_t* __restrict__ cr,
                                                       const bf16_t* __restrict__ ci, bf16_t* __restrict__ abT,
                                                       u32x4* __restrict__ zero_words DBGARG) {
    extern __shared__ __attribute__((aligned(16))) char smem[];
    const int tid = threadIdx.x, w = __builtin_amdgcn_readfirstlane(tid >> 6), lane = tid & 63, fr = lane & 15, fq = lane >> 4;
    const int b = blockIdx.x;
    const int et = ((b >> 2) & 1) * 4 + ((b >> 3) & 3), st = (b & 3) * 8 + (b >> 5);
    char* xs = smem;
    char* hs = smem + 65536;
    STAMP(0, 0);
    if (w >= 8) {
        const int lw = w - 8;
        const bf16_t* hsrc = (et < 4) ? (hr + (size_t)(et * 128) * D_) : (hi + (size_t)((et - 4) * 128) * D_);
        const bf16_t* hp[8];
#pragma unroll
        for (int i = 0; i < 8; ++i) {
            const int rho = (lw * 8 + i) * 4 + fq;
            const int el = (rho >> 6) * 64 + 4 * (rho & 15) + ((rho >> 4) & 3);
            hp[i] = hsrc + (size_t)el * D_ + (fr ^ (rho & 15)) * 8;
        }
        const int xcd = b & 7, j = b >> 3, ks = xcd >> 1;
        const char* pf0 = (const char*)(ks < 2 ? cr : ci) + (size_t)((xcd & 1) * 1024 + j * 32 + lw * 8 + fq) * 4096 + (ks & 1) * 2048 + fr * 128;
        const char* pf1 = pf0 + 4 * 4096;
        unsigned d0, d1;
        __builtin_amdgcn_s_sleep(K1_SLEEP);
#pragma unroll
        for (int c = 0; c < 3; ++c)
#pragma unroll
            for (int i = 0; i < 8; ++i) GLDS(hp[i] + c * 128, hs + c * 32768 + (lw * 8 + i) * 1024);
        u32x4 h3[8];
#pragma unroll
        for (int i = 0; i < 8; ++i)
            asm volatile("global_load_dwordx4 %0, %1, off offset:768" : "=v"(h3[i]) : "v"(hp[i]) : "memory");
        asm volatile("global_load_dword %0, %2, off\n\tglobal_load_dword %1, %3, off" : "=&v"(d0), "=&v"(d1) : "v"(pf0), "v"(pf1) : "memory");
        constexpr int PX = 2;
        WAIT_V(24 + PX); BAR();
        WAIT_V(16 + PX); BAR();
        WAIT_V(PX);
        asm volatile("" : "+v"(h3[0]), "+v"(h3[1]), "+v"(h3[2]), "+v"(h3[3]), "+v"(h3[4]), "+v"(h3[5]), "+v"(h3[6]), "+v"(h3[7]));
#pragma unroll
        for (int i = 0; i < 8; ++i) *(u32x4*)(hs + (lw * 8 + i) * 1024 + lane * 16) = h3[i];
        WAIT_L0(); BAR();
        BAR();
        asm volatile("" :: "v"(d0), "v"(d1));
        return;
    }
    const int s0 = st * 64;
#pragma unroll
    for (int i = 0; i < 8; ++i) {
        const int r = w * 8 + i;
        const int key = ((r >> 3) & 3) * 4 + (r & 3);
        GLDS(x + (size_t)(s0 + r) * D_ + (lane ^ key) * 8, xs + r * 1024);
    }
    WAIT_V(0);
    STAMP(0, 1);
#pragma unroll
    for (int g = 0; g < 2; ++g) {
        char* rp = xs + (w * 8 + g * 4 + fq) * 1024 + fr * 16;
        u32x4 xv[4];
#pragma unroll
        for (int i = 0; i < 4; ++i) xv[i] = *(const u32x4*)(rp + i * 256);
        float v[32];
#pragma unroll
        for (int i = 0; i < 4; ++i)
#pragma unroll
            for (int j = 0; j < 4; ++j) { v[i * 8 + j * 2] = bflo(xv[i][j]); v[i * 8 + j * 2 + 1] = bfhi(xv[i][j]); }
        float s0_ = 0.f, s1_ = 0.f;
#pragma unroll
        for (int j = 0; j < 32; j += 2) { s0_ += v[j]; s1_ += v[j + 1]; }
        const float mean = row16_sum(s0_ + s1_) * (1.0f / D_);
        float q0 = 0.f, q1 = 0.f;
#pragma unroll
        for (int j = 0; j < 32; j += 2) { v[j] -= mean; v[j + 1] -= mean; q0 += v[j] * v[j]; q1 += v[j + 1] * v[j + 1]; }
        const float var = row16_sum(q0 + q1) * (1.0f / D_);
        const float rstd = __builtin_amdgcn_rsqf(var + 1e-5f);
#pragma unroll
        for (int i = 0; i < 4; ++i) {
            u32x4 o;
#pragma unroll
            for (int j = 0; j < 4; ++j) o[j] = pack2(v[i * 8 + j * 2] * rstd, v[i * 8 + j * 2 + 1] * rstd);
            *(u32x4*)(rp + i * 256) = o;
        }
    }
    STAMP(0, 2);
    WAIT_L0(); BAR();
    STAMP(0, 3);

    const int sh = w >> 2, nq = w & 3;
    f32x4 acc[2][2];
#pragma unroll
    for (int m = 0; m < 2; ++m)
#pragma unroll
        for (int n = 0; n < 2; ++n) acc[m][n] = (f32x4){0.f, 0.f, 0.f, 0.f};
    const char* ap = xs + (sh * 32 + (fr >> 2) * 8 + (fr & 3)) * 1024;
    const char* bp = hs + (nq * 32 + fr) * 256;
    auto chunk = [&](int c, int buf) {
        bf16x8 a[4][2], bb[4][2];
#pragma unroll
        for (int kk = 0; kk < 4; ++kk) {
            const int ca = ((c * 16 + kk * 4 + fq) ^ fr) * 16;
            const int cb = ((kk * 4 + fq) ^ fr) * 16;
#pragma unroll
            for (int m = 0; m < 2; ++m) a[kk][m] = *(const bf16x8*)(ap + m * 4096 + ca);
#pragma unroll
            for (int n = 0; n < 2; ++n) bb[kk][n] = *(const bf16x8*)(bp + buf * 32768 + n * 4096 + cb);
        }
        SCHED0();
#pragma unroll
        for (int kk = 0; kk < 4; ++kk)
#pragma unroll
            for (int m = 0; m < 2; ++m)
#pragma unroll
                for (int n = 0; n < 2; ++n)
                    acc[m][n] = __builtin_amdgcn_mfma_f32_16x16x32_bf16(a[kk][m], bb[kk][n], acc[m][n], 0, 0, 0);
        SCHED0();
    };
    chunk(0, 0);
    BAR();
    chunk(1, 1);
    BAR();
    chunk(2, 2);
    chunk(3, 0);
    STAMP(0, 4);
    const float sgn = (et < 4) ? 1.0f : -1.0f;
    char* img = hs + 32768;
#pragma unroll
    for (int n = 0; n < 2; ++n) {
        u32x4 o;
        o[0] = pack2(sgn * acc[0][n][0], sgn * acc[0][n][1]);
        o[1] = pack2(sgn * acc[0][n][2], sgn * acc[0][n][3]);
        o[2] = pack2(sgn * acc[1][n][0], sgn * acc[1][n][1]);
        o[3] = pack2(sgn * acc[1][n][2], sgn * acc[1][n][3]);
        const int rho = nq * 32 + n * 16 + fr;
        *(u32x4*)(img + rho * 128 + (((sh * 4 + fq) ^ (rho & 7)) * 16)) = o;
    }
    WAIT_L0(); BAR();
    const int blk = ((((et >> 2) * 2 + (st >> 4)) * 4 + (et & 3)) * 16 + (st & 15));
    char* dst = (char*)abT + (size_t)blk * 16384;
#pragma unroll
    for (int i = 0; i < 2; ++i) {
        const u32x4 v = *(const u32x4*)(img + i * 8192 + tid * 16);
        *(u32x4*)(dst + i * 8192 + tid * 16) = v;
    }
    if (tid < 57 && b * 57 + tid < (FG * 512 * 8 + 1024) / 16) zero_words[b * 57 + tid] = (u32x4){0u, 0u, 0u, 0u};
    STAMP(0, 5);
}

constexpr int K2_STAGE_B = 32 * 1024;
__global__ __launch_bounds__(512, 1) void k2_gemm2(const bf16_t* __restrict__ cr, const bf16_t* __restrict__ ci,
                                                   const bf16_t* __restrict__ abT, bf16_t* __restrict__ part,
                                                   const bf16_t* __restrict__ wg, const bf16_t* __restrict__ wu,
                                                   const bf16_t* __restrict__ wd DBGARG) {
    extern __shared__ __attribute__((aligned(16))) char smem[];
    const int tid = threadIdx.x, w = __builtin_amdgcn_readfirstlane(tid >> 6), lane = tid & 63, fr = lane & 15, fq = lane >> 4;
    const int b = blockIdx.x, xcd = b & 7, j = b >> 3;
    const int ks = xcd >> 1, mt = (xcd & 1) * 8 + (j >> 2), nt = j & 3;
    const int t0 = mt * 128, e0 = nt * 128;
    constexpr int NT = 16, NB = K2_NB;
    STAMP(1, 0);
    if (w >= 4) {
        const int lw = w - 4;
        const int srow = lane >> 3, schunk = (lane & 7) ^ (srow & 7);
        const char* gA = (const char*)(ks < 2 ? cr : ci) + (size_t)(t0 + lw * 8 + srow) * 4096 + (ks & 1) * 2048 + schunk * 16;
        const char* gB = (const char*)abT + (size_t)((ks * 4 + nt) * 16) * 16384 + lw * 1024 + lane * 16;
        auto stage = [&](int kt) {
            char* base = smem + (kt % NB) * K2_STAGE_B + lw * 1024;
#pragma unroll
            for (int i = 0; i < 4; ++i) {
                GLDS(gA + (size_t)i * 32 * 4096 + kt * 128, base + i * 4096);
                GLDS(gB + kt * 16384 + i * 4096, base + 16384 + i * 4096);
            }
        };
        stage(0); stage(1);
        WAIT_V(8);
        BAR();
        stage(2);
        int issued = 2;
#pragma unroll
        for (int t = 0; t < NT - 1; ++t) {
            const int allowed = issued - (t + 1);
            if (allowed >= 4) WAIT_V(32); else if (allowed == 3) WAIT_V(24); else if (allowed == 2) WAIT_V(16); else if (allowed == 1) WAIT_V(8); else WAIT_V(0);
            BAR();
            const int lim = (t + NB < NT - 1) ? (t + NB) : (NT - 1);
#pragma unroll
            for (int q = 0; q < 2; ++q) if (issued < lim) { ++issued; stage(issued); }
        }
        {
            const int slot = j * 256 + lw * 64 + lane;
            const char* p;
            if (slot < 5120) {
                const int m = slot / 2560, r = (slot % 2560) / 5, l = slot % 5;
                int off = ((xcd * 448) & ~127) + l * 128; off = off < 3583 ? off : 3583;
                p = (const char*)(m ? wu : wg) + (size_t)r * (F_ * 2) + (off & ~3);
            } else {
                int q = slot - 5120; q = q < 1792 ? q : 1791;
                p = (const char*)wd + (size_t)(xcd * 224) * 1024 + q * 128;
            }
            unsigned d0;
            asm volatile("global_load_dword %0, %1, off" : "=&v"(d0) : "v"(p) : "memory");
            asm volatile("" :: "v"(d0));
        }
        return;
    }
    const int wr = w >> 1, wc = w & 1;
    f32x4 acc[4][4];
#pragma unroll
    for (int m = 0; m < 4; ++m)
#pragma unroll
        for (int n = 0; n < 4; ++n) acc[m][n] = (f32x4){0.f, 0.f, 0.f, 0.f};
    const int c0 = ((0 + fq) ^ (fr & 7)) * 16, c1 = ((4 + fq) ^ (fr & 7)) * 16;
    const char* ap = smem + (wr * 64 + fr) * 128;
    const char* bp = smem + 16384 + (wc * 64 + fr) * 128;
    bf16x8 a0[4], b0[4], a1[4], b1[4];
    auto rd = [&](bf16x8* a, bf16x8* bb, int buf, int c) {
#pragma unroll
        for (int m = 0; m < 4; ++m) { a[m] = *(const bf16x8*)(ap + buf * K2_STAGE_B + m * 2048 + c); bb[m] = *(const bf16x8*)(bp + buf * K2_STAGE_B + m * 2048 + c); }
    };
    auto mm = [&](bf16x8* a, bf16x8* bb) {
#pragma unroll
        for (int m = 0; m < 4; ++m)
#pragma unroll
            for (int n = 0; n < 4; ++n)
                acc[m][n] = __builtin_amdgcn_mfma_f32_16x16x32_bf16(a[m], bb[n], acc[m][n], 0, 0, 0);
    };
#define INTERLEAVE() do { _Pragma("unroll") for (int q_ = 0; q_ < 8; ++q_) { __builtin_amdgcn_sched_group_barrier(0x008, 1, 0); __builtin_amdgcn_sched_group_barrier(0x100, 1, 0); } __builtin_amdgcn_sched_group_barrier(0x008, 8, 0); SCHED0(); } while (0)
    BAR();
    STAMP(1, 1);
    rd(a0, b0, 0, c0);
    SCHED0();
#pragma unroll
    for (int t = 0; t < NT; ++t) {
        rd(a1, b1, t % NB, c1);
        mm(a0, b0);
        INTERLEAVE();
        if (t < NT - 1) {
            WAIT_L0();
            BAR();
            rd(a0, b0, (t + 1) % NB, c0);
            mm(a1, b1);
            INTERLEAVE();
        } else {
            mm(a1, b1);
        }
    }
    STAMP(1, 2);
    bf16_t* P = part + (size_t)ks * S_ * D_ + (size_t)(t0 + wr * 64 + fq * 4) * D_ + e0 + wc * 64 + fr * 4;
#pragma unroll
    for (int m = 0; m < 4; ++m)
#pragma unroll
        for (int r = 0; r < 4; ++r) {
            u32x2 o;
            o[0] = pack2(acc[m][0][r], acc[m][1][r]);
            o[1] = pack2(acc[m][2][r], acc[m][3][r]);
            *(u32x2*)(P + (size_t)(m * 16 + r) * D_) = o;
        }
    STAMP(1, 3);
}

#define RLX_AGENT __ATOMIC_RELAXED, __HIP_MEMORY_SCOPE_AGENT
constexpr unsigned SPIN_CAP = 1u << 22;
__global__ __launch_bounds__(512, 2) void k34(const bf16_t* __restrict__ part, const bf16_t* __restrict__ x,
                                              const bf16_t* __restrict__ wr_, const bf16_t* __restrict__ wg,
                                              const bf16_t* __restrict__ wu, const bf16_t* __restrict__ wd,
                                              unsigned* cand, u64* ygran, bf16_t* cand_rows, bf16_t* out DBGARG) {
    __shared__ __attribute__((aligned(16))) float s_buf[8][512 + 16];
    __shared__ float s_hid[32];
    __shared__ unsigned s_key[8];
    const int tid = threadIdx.x, w = __builtin_amdgcn_readfirstlane(tid >> 6), lane = tid & 63;
    const __amdgpu_buffer_rsrc_t rs_rows = __builtin_amdgcn_make_buffer_rsrc((void*)cand_rows, 0, 256 * 2048, 0x00020000);
    if (blockIdx.x >= FG) {
        STAMP(2, 0);
        const int rb = blockIdx.x - FG;
        const int row0 = rb * 16 + w * 2;
        const size_t o = (size_t)row0 * D_ + lane * 8;
        u32x4 p[2][4], xv[2];
#pragma unroll
        for (int r = 0; r < 2; ++r) {
#pragma unroll
            for (int k = 0; k < 4; ++k) p[r][k] = *(const u32x4*)(part + (size_t)k * S_ * D_ + o + r * D_);
            xv[r] = *(const u32x4*)(x + o + r * D_);
        }
        const u32x4 wv = *(const u32x4*)(wr_ + lane * 8);
        float h[2][8], s[2];
        u32x4 hv[2], nv[2], ov[2];
#pragma unroll
        for (int r = 0; r < 2; ++r) {
            s[r] = 0.f;
#pragma unroll
            for (int j = 0; j < 4; ++j) {
                const float y0 = (bflo(p[r][0][j]) + bflo(p[r][1][j])) + (bflo(p[r][2][j]) + bflo(p[r][3][j]));
                const float y1 = (bfhi(p[r][0][j]) + bfhi(p[r][1][j])) + (bfhi(p[r][2][j]) + bfhi(p[r][3][j]));
                hv[r][j] = pack2(bflo(xv[r][j]) + y0, bfhi(xv[r][j]) + y1);
                h[r][2 * j] = bflo(hv[r][j]); h[r][2 * j + 1] = bfhi(hv[r][j]);
                s[r] += h[r][2 * j] + h[r][2 * j + 1];
            }
        }
        s[0] = row16_sum(s[0]); s[1] = row16_sum(s[1]);
        s[0] = rows_total(s[0]); s[1] = rows_total(s[1]);
        STAMP(2, 1);
        float q[2], rr[2], d[2][8];
#pragma unroll
        for (int r = 0; r < 2; ++r) {
            const float mean = s[r] * (1.0f / D_);
            q[r] = 0.f; rr[r] = 0.f;
#pragma unroll
            for (int j = 0; j < 8; ++j) {
                d[r][j] = h[r][j] - mean; q[r] += d[r][j] * d[r][j];
                const float ww = (j & 1) ? bfhi(wv[j >> 1]) : bflo(wv[j >> 1]);
                rr[r] += d[r][j] * ww;
            }
        }
#pragma unroll
        for (int r = 0; r < 2; ++r) { q[r] = row16_sum(q[r]); rr[r] = row16_sum(rr[r]); }
        unsigned key[2];
#pragma unroll
        for (int r = 0; r < 2; ++r) {
            q[r] = rows_total(q[r]); rr[r] = rows_total(rr[r]);
            const float rstd = __builtin_amdgcn_rsqf(q[r] * (1.0f / D_) + 1e-5f);
            const unsigned rbits = pack2(rr[r] * rstd, 0.f) & 0xffffu;
            unsigned ord = (rbits & 0x8000u) ? (~rbits & 0xffffu) : (rbits | 0x8000u);
            if ((rbits & 0x7fffu) > 0x7f80u) ord = 0u;
            key[r] = 0x80000000u | (ord << 11) | (unsigned)(2047 - (row0 + r));
#pragma unroll
            for (int j = 0; j < 4; ++j) {
                nv[r][j] = pack2(d[r][2 * j] * rstd, d[r][2 * j + 1] * rstd);
                ov[r][j] = pack2(h[r][2 * j] + bflo(nv[r][j]), h[r][2 * j + 1] + bfhi(nv[r][j]));
            }
        }
        const int sel = key[1] > key[0] ? 1 : 0;
        const unsigned kw = sel ? key[1] : key[0];
        if (lane == 0) s_key[w] = kw;
        *(u32x4*)(out + o + (1 - sel) * D_) = sel ? ov[0] : ov[1];
        STAMP(2, 2);
        __syncthreads();
        unsigned best = s_key[0];
#pragma unroll
        for (int k = 1; k < 8; ++k) { const unsigned v = s_key[k]; best = v > best ? v : best; }
        const u32x4 ovw = sel ? ov[1] : ov[0];
        if (kw == best) {
            __builtin_amdgcn_raw_buffer_store_b128(sel ? nv[1] : nv[0], rs_rows, rb * 2048 + lane * 16, 0, 16);
            __builtin_amdgcn_raw_buffer_store_b128(sel ? hv[1] : hv[0], rs_rows, rb * 2048 + 1024 + lane * 16, 0, 16);
            const __amdgpu_buffer_rsrc_t rs_out = __builtin_amdgcn_make_buffer_rsrc((void*)out, 0, S_ * D_ * 2, 0x00020000);
            __builtin_amdgcn_raw_buffer_store_b128(ovw, rs_out, (int)((o + sel * D_) * 2), 0, 16);
            WAIT_V(0);
            if (lane == 0) __hip_atomic_store(cand + rb, kw, RLX_AGENT);
        } else {
            *(u32x4*)(out + o + sel * D_) = ovw;
        }
        STAMP(2, 3);
        return;
    }
    STAMP(3, 0);
    const int fb = blockIdx.x, f0 = ((fb & 7) * 7 + (fb >> 3)) * 32;
    const int c = lane & 3, rl = lane >> 2;
    u32x4 g[4], u[4], dn[4];
#pragma unroll
    for (int i = 0; i < 4; ++i) {
        const int d = (i * 8 + w) * 16 + rl;
        g[i] = *(const u32x4*)(wg + (size_t)d * F_ + f0 + c * 8);
        u[i] = *(const u32x4*)(wu + (size_t)d * F_ + f0 + c * 8);
    }
#pragma unroll
    for (int i = 0; i < 4; ++i) dn[i] = *(const u32x4*)(wd + (size_t)(f0 + i * 8 + w) * D_ + lane * 8);
    if (w == 0) {
        unsigned best = 0;
        for (unsigned spins = 0; spins < SPIN_CAP; ++spins) {
            const u64 cv = __hip_atomic_load((const u64*)cand + lane, RLX_AGENT);
            const unsigned c0 = (unsigned)cv, c1 = (unsigned)(cv >> 32);
            const bool ok = (c0 & c1 & 0x80000000u) != 0u;
            if (__all(ok)) {
                best = wave_umax(c0 > c1 ? c0 : c1);
                break;
            }
            __builtin_amdgcn_s_sleep(1);
        }
        if (lane == 0) s_key[0] = best;
    }
    __syncthreads();
    const unsigned best = s_key[0];
    const int row = 2047 - (int)(best & 2047u);
    const int rb = row >> 4;
    STAMP(3, 1);
    float xv[4];
#pragma unroll
    for (int i = 0; i < 4; ++i) {
        const int d = (i * 8 + w) * 16 + rl;
        const unsigned pr = __builtin_amdgcn_raw_buffer_load_b32(rs_rows, rb * 2048 + (d >> 1) * 4, 0, 16);
        xv[i] = (d & 1) ? bfhi(pr) : bflo(pr);
    }
    unsigned hn_h = 0;
    if (fb < 8 && tid < 64) hn_h = __builtin_amdgcn_raw_buffer_load_b32(rs_rows, rb * 2048 + (tid >> 5) * 1024 + fb * 128 + (tid & 31) * 4, 0, 16);
    float ag[8], au[8];
#pragma unroll
    for (int j = 0; j < 8; ++j) { ag[j] = 0.f; au[j] = 0.f; }
#pragma unroll
    for (int i = 0; i < 4; ++i) {
#pragma unroll
        for (int j = 0; j < 4; ++j) {
            ag[2 * j] += xv[i] * bflo(g[i][j]); ag[2 * j + 1] += xv[i] * bfhi(g[i][j]);
            au[2 * j] += xv[i] * bflo(u[i][j]); au[2 * j + 1] += xv[i] * bfhi(u[i][j]);
        }
    }
    STAMP(3, 2);
#pragma unroll
    for (int j = 0; j < 8; ++j) {
        ag[j] = dpp_add<0x124>(ag[j]); ag[j] = dpp_add<0x128>(ag[j]);
        au[j] = dpp_add<0x124>(au[j]); au[j] = dpp_add<0x128>(au[j]);
    }
    if ((lane & 12) == 0) {
        float* rp = &s_buf[w][(lane >> 4) * 64 + c * 16];
        *(f32x4*)(rp) = (f32x4){ag[0], ag[1], ag[2], ag[3]};
        *(f32x4*)(rp + 4) = (f32x4){ag[4], ag[5], ag[6], ag[7]};
        *(f32x4*)(rp + 8) = (f32x4){au[0], au[1], au[2], au[3]};
        *(f32x4*)(rp + 12) = (f32x4){au[4], au[5], au[6], au[7]};
    }
    __syncthreads();
    if (w == 0) {
        float s = 0.f;
#pragma unroll
        for (int k = 0; k < 8; ++k)
#pragma unroll
            for (int r4 = 0; r4 < 4; ++r4) s += s_buf[k][r4 * 64 + lane];
        const float up = __int_as_float(__builtin_amdgcn_update_dpp(0, __float_as_int(s), 0x128, 0xf, 0xf, true));
        if ((lane & 8) == 0) {
            const float gb = rbf(s);
            const float sg = rbf(gb / (1.0f + __expf(-gb)));
            s_hid[(lane >> 4) * 8 + (lane & 7)] = rbf(sg * rbf(up));
        }
    }
    __syncthreads();
    STAMP(3, 3);
    float ay[8];
#pragma unroll
    for (int j = 0; j < 8; ++j) ay[j] = 0.f;
#pragma unroll
    for (int i = 0; i < 4; ++i) {
        const float hv = s_hid[i * 8 + w];
#pragma unroll
        for (int j = 0; j < 4; ++j) { ay[2 * j] += hv * bflo(dn[i][j]); ay[2 * j + 1] += hv * bfhi(dn[i][j]); }
    }
    *(f32x4*)&s_buf[w][lane * 8] = (f32x4){ay[0], ay[1], ay[2], ay[3]};
    *(f32x4*)&s_buf[w][lane * 8 + 4] = (f32x4){ay[4], ay[5], ay[6], ay[7]};
    __syncthreads();
    {
        float y = 0.f;
#pragma unroll
        for (int k = 0; k < 8; ++k) y += s_buf[k][tid];
        __hip_atomic_store(ygran + (size_t)fb * D_ + tid, (1ull << 32) | (u64)__float_as_uint(y), RLX_AGENT);
    }
    STAMP(3, 4);
    if (fb >= 8) return;
    {
        float s = 0.f;
        for (unsigned spins = 0; spins < SPIN_CAP; ++spins) {
            u64 v[7]; bool ok = true;
#pragma unroll
            for (int k = 0; k < 7; ++k) v[k] = __hip_atomic_load(ygran + (size_t)(w * 7 + k) * D_ + fb * 64 + lane, RLX_AGENT);
            s = 0.f;
#pragma unroll
            for (int k = 0; k < 7; ++k) { ok &= (v[k] >> 32) == 1ull; s += __uint_as_float((unsigned)v[k]); }
            if (__all(ok)) break;
            __builtin_amdgcn_s_sleep(1);
        }
        __syncthreads();
        s_buf[w][lane] = s;
        if (tid < 64) s_buf[0][256 + tid] = __uint_as_float(hn_h);
        __syncthreads();
        STAMP(3, 5);
        if (tid < 32) {
            float y0 = 0.f, y1 = 0.f;
#pragma unroll
            for (int k = 0; k < 8; ++k) { y0 += s_buf[k][2 * tid]; y1 += s_buf[k][2 * tid + 1]; }
            const unsigned ord = (best >> 11) & 0xffffu;
            const unsigned rbits = (ord & 0x8000u) ? (ord & 0x7fffu) : (~ord & 0xffffu);
            const float br = __uint_as_float(rbits << 16);
            const float rs = rbf(1.0f / (1.0f + __expf(-br)));
            const unsigned nw = __float_as_uint(s_buf[0][256 + tid]), hw = __float_as_uint(s_buf[0][256 + 32 + tid]);
            const float u0 = rbf(rbf(y0) * rs), u1 = rbf(rbf(y1) * rs);
            const float o0 = bflo(hw) + rbf(bflo(nw) + u0), o1 = bfhi(hw) + rbf(bfhi(nw) + u1);
            __hip_atomic_store((unsigned*)(out + (size_t)row * D_ + fb * 64 + 2 * tid), pack2(o0, o1), RLX_AGENT);
        }
        STAMP(3, 6);
    }
}

extern "C" void kernel_launch(void* const* d_in, const int* in_sizes, int n_in, void* d_out, int out_size, void* d_ws,
                              size_t ws_size, hipStream_t stream) {
    const bf16_t* x = (const bf16_t*)d_in[0];
    const bf16_t* cr = (const bf16_t*)d_in[1];
    const bf16_t* ci = (const bf16_t*)d_in[2];
    const bf16_t* hr = (const bf16_t*)d_in[3];
    const bf16_t* hi = (const bf16_t*)d_in[4];
    const bf16_t* wr = (const bf16_t*)d_in[6];
    const bf16_t* wg = (const bf16_t*)d_in[7];
    const bf16_t* wu = (const bf16_t*)d_in[8];
    const bf16_t* wd = (const bf16_t*)d_in[9];
    bf16_t* out = (bf16_t*)d_out;
    char* ws = (char*)d_ws;
    bf16_t* abT = (bf16_t*)(ws);
    bf16_t* part = (bf16_t*)(ws + (4u << 20));
    char* small = ws + (20u << 20);
    unsigned* cand = (unsigned*)(small);
    u64* ygran = (u64*)(small + 1024);
    bf16_t* cand_rows = (bf16_t*)(small + (1u << 20));

    (void)hipFuncSetAttribute((const void*)k1_ln_gemm1, hipFuncAttributeMaxDynamicSharedMemorySize, 160 * 1024);
    (void)hipFuncSetAttribute((const void*)k2_gemm2, hipFuncAttributeMaxDynamicSharedMemorySize, K2_NB * K2_STAGE_B);
    k1_ln_gemm1<<<256, 768, 160 * 1024, stream>>>(x, hr, hi, cr, ci, abT, (u32x4*)small DBGPASS);
    k2_gemm2<<<256, 512, K2_NB * K2_STAGE_B, stream>>>(cr, ci, abT, part, wg, wu, wd DBGPASS);
    k34<<<128 + FG, 512, 0, stream>>>(part, x, wr, wg, wu, wd, cand, ygran, cand_rows, out DBGPASS);
}
```

```cpp
#include <hip/hip_runtime.h>
#include <stdint.h>

typedef unsigned short bf16_t;
typedef short bf16x8 __attribute__((ext_vector_type(8)));
typedef float f32x4 __attribute__((ext_vector_type(4)));
typedef float f32x2 __attribute__((ext_vector_type(2)));
typedef unsigned u32x4 __attribute__((ext_vector_type(4)));
typedef unsigned u32x2 __attribute__((ext_vector_type(2)));
typedef unsigned long long u64;

constexpr int S_ = 2048, D_ = 512, F_ = 1792;
constexpr int FG = 56;
constexpr unsigned SENT = 0xFFFFFFFFu;
constexpr int HO_ABT = 0, HO_PART = 4 << 20, HO_EPOCH = 12 << 20, HO_FLAGA = HO_EPOCH + 4096, HO_FLAGP = HO_FLAGA + 65536,
              HO_KEY = HO_FLAGP + 16384, HO_WIN = HO_KEY + 16384, HO_HN = HO_WIN + 4096, HO_Y = HO_HN + 256 * 1024,
              HO_BYTES = HO_Y + FG * 2048;
constexpr int LDS_BYTES = 160 * 1024;
constexpr int PF_WIN = 4;
constexpr int PF_N = 16;
constexpr int CODE_KB = 20;

__device__ __forceinline__ float bf2f(unsigned short b) { return __uint_as_float(((unsigned)b) << 16); }
__device__ __forceinline__ float bflo(unsigned w) { return __uint_as_float(w << 16); }
__device__ __forceinline__ float bfhi(unsigned w) { return __uint_as_float(w & 0xffff0000u); }
typedef __bf16 bf16x2_t __attribute__((ext_vector_type(2)));
__device__ __forceinline__ unsigned pack2(float lo, float hi) {
    bf16x2_t v; v.x = (__bf16)lo; v.y = (__bf16)hi;
    return __builtin_bit_cast(unsigned, v);
}
__device__ __forceinline__ float rbf(float x) { __bf16 b = (__bf16)x; return (float)b; }

template <int CTRL>
__device__ __forceinline__ float dpp_add(float v) {
    int o = __builtin_amdgcn_update_dpp(0, __float_as_int(v), CTRL, 0xf, 0xf, true);
    return v + __int_as_float(o);
}
__device__ __forceinline__ float row16_sum(float v) {
    v = dpp_add<0xB1>(v); v = dpp_add<0x4E>(v); v = dpp_add<0x141>(v); v = dpp_add<0x140>(v);
    return v;
}
__device__ __forceinline__ float rows_total(float v) {
    float a = __int_as_float(__builtin_amdgcn_readlane(__float_as_int(v), 0));
    float b = __int_as_float(__builtin_amdgcn_readlane(__float_as_int(v), 16));
    float c = __int_as_float(__builtin_amdgcn_readlane(__float_as_int(v), 32));
    float d = __int_as_float(__builtin_amdgcn_readlane(__float_as_int(v), 48));
    return (a + b) + (c + d);
}
__device__ __forceinline__ float wave_sum(float v) { return rows_total(row16_sum(v)); }
template <int CTRL>
__device__ __forceinline__ unsigned dpp_umax(unsigned v) {
    unsigned o = (unsigned)__builtin_amdgcn_update_dpp(0, (int)v, CTRL, 0xf, 0xf, true);
    return v > o ? v : o;
}
__device__ __forceinline__ unsigned wave_umax(unsigned v) {
    v = dpp_umax<0xB1>(v); v = dpp_umax<0x4E>(v); v = dpp_umax<0x141>(v); v = dpp_umax<0x140>(v);
    unsigned a = __builtin_amdgcn_readlane(v, 0), b = __builtin_amdgcn_readlane(v, 16);
    unsigned c = __builtin_amdgcn_readlane(v, 32), d = __builtin_amdgcn_readlane(v, 48);
    a = a > b ? a : b; c = c > d ? c : d;
    return a > c ? a : c;
}
__device__ __forceinline__ unsigned umax3(unsigned a, unsigned b, unsigned c) { a = a > b ? a : b; return a > c ? a : c; }
__device__ __forceinline__ unsigned umax4(const u32x4 v) { return umax3(umax3(v[0], v[1], v[2]), v[3], 0u); }
__device__ __forceinline__ unsigned nosent(unsigned v) { return v < SENT - 1u ? v : SENT - 1u; }

#define WAIT_V(n) asm volatile("s_waitcnt vmcnt(%0)" ::"n"(n) : "memory")
#define WAIT_L0() asm volatile("s_waitcnt lgkmcnt(0)" ::: "memory")
#define BAR() do { asm volatile("" ::: "memory"); __builtin_amdgcn_s_barrier(); asm volatile("" ::: "memory"); } while (0)
#define GLDS(src, dst) __builtin_amdgcn_global_load_lds((const unsigned*)(src), (unsigned*)(dst), 16, 0, 0)
#define GLDSB(src, dst) __builtin_amdgcn_global_load_lds((const unsigned*)(src), (unsigned*)(dst), 16, 0, 16)
#define SCHED0() __builtin_amdgcn_sched_barrier(0)
#define SGB(mask, n) __builtin_amdgcn_sched_group_barrier(mask, n, 0)
#define TOUCH(base, off) asm volatile("s_load_dword s95, %0, %1" ::"s"(base), "n"(off) : "s95")
#define RLX_AGENT __ATOMIC_RELAXED, __HIP_MEMORY_SCOPE_AGENT
constexpr unsigned SPIN_CAP = 1u << 22;
constexpr int STAGE_B = 32 * 1024;

struct RowOut { u32x4 hv, nv, ov; unsigned key; };
__device__ __forceinline__ void row_load(u32x4* p, const __amdgpu_buffer_rsrc_t rs_ho, int row, int lane) {
#pragma unroll
    for (int k = 0; k < 4; ++k) p[k] = __builtin_amdgcn_raw_buffer_load_b128(rs_ho, HO_PART + (k * S_ + row) * (D_ * 2) + lane * 16, 0, 16);
    SCHED0();
}
__device__ __forceinline__ RowOut row_finish(const u32x4* p, int row, const u32x4 xv, const u32x4 wv) {
    RowOut o;
    float h[8], s = 0.f;
#pragma unroll
    for (int j = 0; j < 4; ++j) {
        const float y0 = (bflo(p[0][j]) + bflo(p[1][j])) + (bflo(p[2][j]) + bflo(p[3][j]));
        const float y1 = (bfhi(p[0][j]) + bfhi(p[1][j])) + (bfhi(p[2][j]) + bfhi(p[3][j]));
        o.hv[j] = pack2(bflo(xv[j]) + y0, bfhi(xv[j]) + y1);
        h[2 * j] = bflo(o.hv[j]); h[2 * j + 1] = bfhi(o.hv[j]);
        s += h[2 * j] + h[2 * j + 1];
    }
    const float mean = wave_sum(s) * (1.0f / D_);
    float q = 0.f, rr = 0.f, d[8];
#pragma unroll
    for (int j = 0; j < 8; ++j) {
        d[j] = h[j] - mean; q += d[j] * d[j];
        const float ww = (j & 1) ? bfhi(wv[j >> 1]) : bflo(wv[j >> 1]);
        rr += d[j] * ww;
    }
    q = row16_sum(q); rr = row16_sum(rr);
    q = rows_total(q); rr = rows_total(rr);
    const float rstd = __builtin_amdgcn_rsqf(q * (1.0f / D_) + 1e-5f);
    const unsigned rbits = pack2(rr * rstd, 0.f) & 0xffffu;
    unsigned ord = (rbits & 0x8000u) ? (~rbits & 0xffffu) : (rbits | 0x8000u);
    if ((rbits & 0x7fffu) > 0x7f80u) ord = 0u;
    o.key = (ord << 11) | (unsigned)(2047 - row);
#pragma unroll
    for (int j = 0; j < 4; ++j) {
        o.nv[j] = pack2(d[2 * j] * rstd, d[2 * j + 1] * rstd);
        o.ov[j] = pack2(h[2 * j] + bflo(o.nv[j]), h[2 * j + 1] + bfhi(o.nv[j]));
    }
    return o;
}

__global__ __launch_bounds__(512, 1) void fused(const bf16_t* __restrict__ x, const bf16_t* __restrict__ cr,
                                                const bf16_t* __restrict__ ci, const bf16_t* __restrict__ hr,
                                                const bf16_t* __restrict__ hi, const bf16_t* __restrict__ wr_,
                                                const bf16_t* __restrict__ wg, const bf16_t* __restrict__ wu,
                                                const bf16_t* __restrict__ wd, char* ho, bf16_t* out) {
    extern __shared__ __attribute__((aligned(16))) char smem[];
    const int tid = threadIdx.x, w = __builtin_amdgcn_readfirstlane(tid >> 6), lane = tid & 63, fr = lane & 15, fq = lane >> 4;
    const int b = blockIdx.x, xcd = b & 7, j = b >> 3;
    const int et = ((b >> 2) & 1) * 4 + ((b >> 3) & 3), st = (b & 3) * 8 + (b >> 5);
    const int ks = xcd >> 1, mt = (xcd & 1) * 8 + (j >> 2), nt = j & 3;
    const int t0 = mt * 128, e0 = nt * 128;
    constexpr int NT = 16, NB = 4;
    const bool ffn = b < FG;
    const int f0 = ((b & 7) * 7 + (b >> 3)) * 32;
    const int myrow = t0 + (ks * 4 + nt) * 8 + w;
    const __amdgpu_buffer_rsrc_t rs_ho = __builtin_amdgcn_make_buffer_rsrc((void*)ho, 0, HO_BYTES, 0x00020000);
    const __amdgpu_buffer_rsrc_t rs_out = __builtin_amdgcn_make_buffer_rsrc((void*)out, 0, S_ * D_ * 2, 0x00020000);
    unsigned tag;
#define LOAD_TAG() do { asm volatile("s_load_dword %0, %1, 0x0\n\ts_waitcnt lgkmcnt(0)" : "=s"(tag) : "s"(ho + HO_EPOCH) : "memory"); tag += 1u; } while (0)
    char* xs = smem;
    f32x2* s_stat = (f32x2*)(smem + 147456);
    unsigned* s_hdone = (unsigned*)(smem + 147456 + 512);
    u32x4 g[8], u[8], dn[8];
    u32x4 xv, wv;
    unsigned d2 = 0;
    const char* cbase = (const char*)(ks < 2 ? cr : ci) + (ks & 1) * 2048;
    if (w >= 4) {
        __builtin_amdgcn_s_setprio(3);
#pragma unroll
        for (int i = 0; i < 16; ++i) {
            const int r = (w - 4) * 16 + i;
            const int key = ((r >> 3) & 3) * 4 + (r & 3);
            GLDS(x + (size_t)(st * 64 + r) * D_ + (lane ^ key) * 8, xs + r * 1024);
        }
    }
    const char* pf = cbase + (size_t)((xcd & 1) * 1024 + j * 32 + (w & 3) * 8 + (lane >> 5)) * 4096 + (lane & 31) * 16;
    char* scr = smem + 147456 + 1024 + (w & 3) * 1024;
    if (w >= 4) {
        const int lw = w - 4;
        if (lane < 16) *(u32x2*)(s_stat + lw * 16 + lane) = (u32x2){SENT, SENT};
        if (lane == 16) s_hdone[lw] = SENT;
        LOAD_TAG();
        WAIT_V(0); WAIT_L0();
        BAR();
        {
            const bf16x8 ones = {0x3f80, 0x3f80, 0x3f80, 0x3f80, 0x3f80, 0x3f80, 0x3f80, 0x3f80};
            const char* ap = xs + ((fr >> 2) * 8 + (fr & 3) + (lw & 1) * 4 + (lw >> 1) * 32) * 1024;
            bf16x8 a[16];
#pragma unroll
            for (int kk = 0; kk < 16; ++kk) a[kk] = *(const bf16x8*)(ap + ((kk * 4 + fq) ^ fr) * 16);
            f32x4 sx = {0.f, 0.f, 0.f, 0.f}, sq = {0.f, 0.f, 0.f, 0.f};
#pragma unroll
            for (int kk = 0; kk < 16; ++kk) {
                sx = __builtin_amdgcn_mfma_f32_16x16x32_bf16(a[kk], ones, sx, 0, 0, 0);
                sq = __builtin_amdgcn_mfma_f32_16x16x32_bf16(a[kk], a[kk], sq, 0, 0, 0);
            }
            const int r = fr & 3;
            const float s1 = r == 0 ? sx[0] : r == 1 ? sx[1] : r == 2 ? sx[2] : sx[3];
            const float s2 = r == 0 ? sq[0] : r == 1 ? sq[1] : r == 2 ? sq[2] : sq[3];
            const float mean = s1 * (1.0f / D_);
            const float rstd = __builtin_amdgcn_rsqf(fmaxf(s2 * (1.0f / D_) - mean * mean, 0.f) + 1e-5f);
            if ((fr >> 2) == fq)
                *(u32x2*)(s_stat + (lw >> 1) * 32 + fq * 8 + (lw & 1) * 4 + r) = (u32x2){nosent(__float_as_uint(mean)), nosent(__float_as_uint(rstd))};
        }
        __builtin_amdgcn_s_setprio(0);
#pragma nounroll
        for (unsigned spins = 0; spins < SPIN_CAP; ++spins) {
            asm volatile("" ::: "memory");
            const u32x4 hv = *(const u32x4*)(s_hdone);
            if (__builtin_amdgcn_readfirstlane(umax4(hv)) != SENT) break;
            __builtin_amdgcn_s_sleep(2);
        }
#pragma unroll
        for (int q = 0; q < PF_N; ++q) {
            if (q >= PF_WIN) WAIT_V(PF_WIN - 1);
            GLDS(pf + (q & 3) * 8192 + (((q >> 2) * 512 + mt * 128) & 2047 & ~511), scr);
        }
        WAIT_L0();
    } else {
        {
            char* hs = smem + 65536 + w * 4096;
            const bf16_t* hsrc = (et < 4) ? (hr + (size_t)(et * 128) * D_) : (hi + (size_t)((et - 4) * 128) * D_);
            const char* hp[4];
#pragma unroll
            for (int i = 0; i < 4; ++i) {
                const int rho = w * 32 + i * 8 + (lane >> 3);
                const int el = (rho >> 6) * 64 + 4 * (rho & 15) + ((rho >> 4) & 3);
                hp[i] = (const char*)(hsrc + (size_t)el * D_) + ((lane & 7) ^ (rho & 7)) * 16;
            }
            auto stageH = [&](int kt) {
#pragma unroll
                for (int i = 0; i < 4; ++i) GLDS(hp[i] + kt * 128, hs + (kt % 5) * 16384 + i * 1024);
            };
            if (j < CODE_KB) {
                const char* pc;
                asm volatile("s_getpc_b64 %0" : "=s"(pc));
                pc += 768 + j * 1024 + w * 256;
                TOUCH(pc, 0); TOUCH(pc, 64); TOUCH(pc, 128); TOUCH(pc, 192);
            }
            stageH(0); stageH(1); stageH(2);
            LOAD_TAG();
            f32x4 acc[4][2], hsum[2];
#pragma unroll
            for (int n = 0; n < 2; ++n) {
                hsum[n] = (f32x4){0.f, 0.f, 0.f, 0.f};
#pragma unroll
                for (int m = 0; m < 4; ++m) acc[m][n] = (f32x4){0.f, 0.f, 0.f, 0.f};
            }
            const bf16x8 ones = {0x3f80, 0x3f80, 0x3f80, 0x3f80, 0x3f80, 0x3f80, 0x3f80, 0x3f80};
            const char* ap = xs + ((fr >> 2) * 8 + (fr & 3)) * 1024;
            const char* bp = hs + fr * 128;
            bf16x8 a0[2][4], b0[2][2], a1[2][4], b1[2][2];
            auto rd = [&](bf16x8 (*a)[4], bf16x8 (*bb)[2], int kt) {
#pragma unroll
                for (int kk = 0; kk < 2; ++kk) {
                    const int ca = ((kt * 8 + kk * 4 + fq) ^ fr) * 16;
                    const int cb = ((kk * 4 + fq) ^ (fr & 7)) * 16;
#pragma unroll
                    for (int n = 0; n < 2; ++n) bb[kk][n] = *(const bf16x8*)(bp + (kt % 5) * 16384 + n * 2048 + cb);
#pragma unroll
                    for (int m = 0; m < 4; ++m) a[kk][m] = *(const bf16x8*)(ap + (m & 1) * 4096 + (m >> 1) * 32768 + ca);
                }
            };
            auto mm = [&](bf16x8 (*a)[4], bf16x8 (*bb)[2]) {
#pragma unroll
                for (int kk = 0; kk < 2; ++kk) {
#pragma unroll
                    for (int n = 0; n < 2; ++n) hsum[n] = __builtin_amdgcn_mfma_f32_16x16x32_bf16(ones, bb[kk][n], hsum[n], 0, 0, 0);
#pragma unroll
                    for (int m = 0; m < 4; ++m)
#pragma unroll
                        for (int n = 0; n < 2; ++n)
                            acc[m][n] = __builtin_amdgcn_mfma_f32_16x16x32_bf16(a[kk][m], bb[kk][n], acc[m][n], 0, 0, 0);
                }
            };
#define IL1() do { _Pragma("unroll") for (int q_ = 0; q_ < 4; ++q_) { SGB(0x008, 1); SGB(0x100, 1); SGB(0x008, 1); SGB(0x100, 1); SGB(0x008, 2); SGB(0x100, 1); } SGB(0x008, 4); SCHED0(); } while (0)
            BAR();
            WAIT_V(8);
            rd(a0, b0, 0); SCHED0();
            u32x4 stv[2][4];
#pragma unroll
            for (int t = 0; t < 8; t += 2) {
                WAIT_L0();
                if (t == 0) { stageH(3); stageH(4); }
                if (t == 2) { stageH(7); if (lane == 0) s_hdone[w] = 0u; }
                if (t == 0) WAIT_V(12); else if (t == 2) WAIT_V(16); else if (t == 4) WAIT_V(8); else WAIT_V(0);
                rd(a1, b1, t + 1); mm(a0, b0); IL1();
                WAIT_L0();
                if (t == 0) { stageH(5); stageH(6); }
                if (t + 2 < 8) {
                    if (t == 0) WAIT_V(16); else if (t == 2) WAIT_V(12); else WAIT_V(4);
                    rd(a0, b0, t + 2); mm(a1, b1); IL1();
                } else {
                    {
                        const u32x4 sv = {SENT, SENT, SENT, SENT};
                        if (w == 0) __builtin_amdgcn_raw_buffer_store_b128(sv, rs_ho, HO_HN + (myrow >> 3) * 1024 + lane * 16, 0, 16);
                        if (w == 1 && lane == 0) __builtin_amdgcn_raw_buffer_store_b128(sv, rs_ho, HO_KEY + b * 64, 0, 16);
                        if (w == 1 && lane == 1 && b < 8) __builtin_amdgcn_raw_buffer_store_b128(sv, rs_ho, HO_WIN + b * 256, 0, 16);
                        if (w >= 2 && ffn) __builtin_amdgcn_raw_buffer_store_b128(sv, rs_ho, HO_Y + b * 2048 + (w - 2) * 1024 + lane * 16, 0, 16);
                    }
#pragma nounroll
                    for (unsigned spins = 0; spins < SPIN_CAP; ++spins) {
                        unsigned mx = 0u;
                        asm volatile("" ::: "memory");
#pragma unroll
                        for (int p = 0; p < 2; ++p)
#pragma unroll
                            for (int q = 0; q < 4; ++q) { stv[p][q] = *(const u32x4*)(s_stat + p * 32 + fq * 8 + q * 2); mx = umax3(mx, umax4(stv[p][q]), 0u); }
                        if (__all(mx != SENT)) break;
                    }
                    mm(a1, b1);
                }
            }
            const float sgn = (et < 4) ? 1.0f : -1.0f;
            char* img = hs + 3 * 16384;
#pragma unroll
            for (int n = 0; n < 2; ++n) {
                const float hsn = hsum[n][0];
#pragma unroll
                for (int p = 0; p < 2; ++p) {
                    u32x4 o;
#pragma unroll
                    for (int q = 0; q < 4; ++q) {
                        const f32x4 a = acc[2 * p + (q >> 1)][n];
                        const float v0 = (a[(q & 1) * 2] - __uint_as_float(stv[p][q][0]) * hsn) * (__uint_as_float(stv[p][q][1]) * sgn);
                        const float v1 = (a[(q & 1) * 2 + 1] - __uint_as_float(stv[p][q][2]) * hsn) * (__uint_as_float(stv[p][q][3]) * sgn);
                        o[q] = pack2(v0, v1);
                    }
                    *(u32x4*)(img + (n * 16 + fr) * 128 + (((p * 4 + fq) ^ (fr & 7)) * 16)) = o;
                }
            }
            const int blk = ((((et >> 2) * 2 + (st >> 4)) * 4 + (et & 3)) * 16 + (st & 15));
#pragma unroll
            for (int i = 0; i < 4; ++i)
                __builtin_amdgcn_raw_buffer_store_b128(*(const u32x4*)(img + i * 1024 + lane * 16), rs_ho, HO_ABT + blk * 16384 + w * 4096 + i * 1024 + lane * 16, 0, 16);
            WAIT_V(0);
            if (lane == 0) __hip_atomic_store((unsigned*)(ho + HO_FLAGA + (blk * 4 + w) * 64), tag, RLX_AGENT);
            WAIT_L0();
        }
    }
    BAR();
    if (w >= 4) {
        const int lw = w - 4;
        const int srow = lane >> 3, schunk = (lane & 7) ^ (srow & 7);
        const char* gA = cbase + (size_t)(t0 + lw * 8 + srow) * 4096 + schunk * 16;
        const char* gB = ho + HO_ABT + (size_t)((ks * 4 + nt) * 16) * 16384 + lw * 4096 + lane * 16;
        auto stageA = [&](int i) {
            const int kk = (mt + i) & 15;
#pragma unroll
            for (int q = 0; q < 4; ++q) GLDS(gA + (size_t)q * 32 * 4096 + kk * 128, smem + ((i + 3) & 3) * STAGE_B + lw * 1024 + q * 4096);
        };
        auto stageB = [&](int i) {
            const int kk = (mt + i) & 15;
#pragma unroll
            for (int q = 0; q < 4; ++q) GLDSB(gB + kk * 16384 + q * 1024, smem + ((i + 3) & 3) * STAGE_B + 16384 + lw * 4096 + q * 1024);
        };
        stageA(0); stageA(1); stageA(2); stageA(3);
        WAIT_V(12);
        BAR();
        {
            const unsigned* fp = (const unsigned*)(ho + HO_FLAGA + (((ks * 4 + nt) * 16 + (lane & 15)) * 4 + lw) * 64);
#pragma nounroll
            for (unsigned spins = 0; spins < SPIN_CAP; ++spins) {
                unsigned v = tag;
                if (lane < 16) v = __hip_atomic_load(fp, RLX_AGENT);
                if (__all(v == tag)) break;
                __builtin_amdgcn_s_sleep(1);
            }
        }
        asm volatile("" ::: "memory");
        const char* pf2;
        if (lane < 32) pf2 = (const char*)((lane & 16) ? wu : wg) + (size_t)(j * 16 + lw * 4 + ((lane >> 2) & 3)) * (F_ * 2) + ((xcd * 448) & ~127) + (lane & 3) * 128;
        else pf2 = (const char*)wd + (size_t)(xcd * 224 + j * 7) * 1024 + lw * 1792 + ((lane - 32) < 14 ? (lane - 32) : 13) * 128;
        stageB(1); stageB(2); stageB(3);
        WAIT_V(8);  BAR(); stageA(4); stageB(4);
        WAIT_V(12); BAR(); stageA(5); stageB(5);
#pragma nounroll
        for (int i = 6; i < NT; ++i) {
            WAIT_V(16);
            BAR();
            stageA(i); stageB(i);
        }
        WAIT_V(16); BAR();
        asm volatile("global_load_dword %0, %1, off" : "=&v"(d2) : "v"(pf2) : "memory");
        WAIT_V(9);  BAR();
        WAIT_V(1);  BAR();
        BAR();
        xv = *(const u32x4*)(x + (size_t)myrow * D_ + lane * 8);
        wv = *(const u32x4*)(wr_ + lane * 8);
        SCHED0();
    } else {
        const int wr = w >> 1, wc = w & 1;
        f32x4 acc[4][4];
#pragma unroll
        for (int m = 0; m < 4; ++m)
#pragma unroll
            for (int n = 0; n < 4; ++n) acc[m][n] = (f32x4){0.f, 0.f, 0.f, 0.f};
        const int c0 = ((0 + fq) ^ (fr & 7)) * 16, c1 = ((4 + fq) ^ (fr & 7)) * 16;
        const char* ap = smem + (wr * 64 + fr) * 128;
        const char* bp = smem + 16384 + (wc * 64 + fr) * 128;
        bf16x8 a0[4], b0[4], a1[4], b1[4];
        auto rd = [&](bf16x8* a, bf16x8* bb, int buf, int c) {
#pragma unroll
            for (int m = 0; m < 4; ++m) { a[m] = *(const bf16x8*)(ap + buf * STAGE_B + m * 2048 + c); bb[m] = *(const bf16x8*)(bp + buf * STAGE_B + m * 2048 + c); }
        };
        auto mm = [&](bf16x8* a, bf16x8* bb) {
#pragma unroll
            for (int m = 0; m < 4; ++m)
#pragma unroll
                for (int n = 0; n < 4; ++n)
                    acc[m][n] = __builtin_amdgcn_mfma_f32_16x16x32_bf16(a[m], bb[n], acc[m][n], 0, 0, 0);
        };
#define IL2() do { _Pragma("unroll") for (int q_ = 0; q_ < 8; ++q_) { SGB(0x008, 1); SGB(0x100, 1); } SGB(0x008, 8); SCHED0(); } while (0)
        BAR();
        rd(a0, b0, 3, c0);
        SCHED0();
        xv = *(const u32x4*)(x + (size_t)myrow * D_ + lane * 8);
        wv = *(const u32x4*)(wr_ + lane * 8);
        SCHED0();
#pragma nounroll
        for (int tt = 0; tt < NT / NB; ++tt) {
#pragma unroll
            for (int u = 0; u < NB; ++u) {
                rd(a1, b1, (u + 3) % NB, c1);
                mm(a0, b0);
                IL2();
                WAIT_L0();
                BAR();
                rd(a0, b0, u, c0);
                mm(a1, b1);
                IL2();
            }
        }
        const int po = ((ks * S_ + t0 + wr * 64 + fq * 4) * D_ + e0 + wc * 64 + fr * 4) * 2;
#pragma unroll
        for (int m = 0; m < 4; ++m)
#pragma unroll
            for (int r = 0; r < 4; ++r) {
                u32x2 o;
                o[0] = pack2(acc[m][0][r], acc[m][1][r]);
                o[1] = pack2(acc[m][2][r], acc[m][3][r]);
                __builtin_amdgcn_raw_buffer_store_b64(o, rs_ho, HO_PART + po + (m * 16 + r) * (D_ * 2), 0, 16);
            }
        WAIT_V(0);
    }
    BAR();
    unsigned* s_key = (unsigned*)smem;
    float* s_buf = (float*)(smem + 1024);
    unsigned* s_row = (unsigned*)(smem + 32768);
    if (ffn && w >= 4) {
        const int lw = w - 4, c = lane & 3, rl = lane >> 2;
#pragma unroll
        for (int i = 0; i < 8; ++i) {
            const int d = (i * 4 + lw) * 16 + rl;
            g[i] = *(const u32x4*)(wg + (size_t)d * F_ + f0 + c * 8);
            u[i] = *(const u32x4*)(wu + (size_t)d * F_ + f0 + c * 8);
        }
        SCHED0();
    }
    if (tid == 0) __hip_atomic_store((unsigned*)(ho + HO_FLAGP + (mt * 16 + ks * 4 + nt) * 64), tag, RLX_AGENT);
    if (ffn && w < 4) {
#pragma unroll
        for (int i = 0; i < 8; ++i) dn[i] = *(const u32x4*)(wd + (size_t)(f0 + i * 4 + w) * D_ + lane * 8);
        SCHED0();
    }
    if (w == 0) {
        const unsigned* fp = (const unsigned*)(ho + HO_FLAGP + (mt * 16 + (lane & 15)) * 64);
        __builtin_amdgcn_s_sleep(14);
#pragma nounroll
        for (unsigned spins = 0; spins < SPIN_CAP; ++spins) {
            unsigned v = tag;
            if (lane < 16) v = __hip_atomic_load(fp, RLX_AGENT);
            if (__all(v == tag)) break;
            __builtin_amdgcn_s_sleep(1);
        }
    }
    asm volatile("" : "+v"(xv), "+v"(wv));
    WAIT_V(0);
    asm volatile("" :: "v"(d2));
    BAR();
    u32x4 prow[4];
    row_load(prow, rs_ho, myrow, lane);
    const RowOut ro = row_finish(prow, myrow, xv, wv);
    if (lane == 0) s_key[w] = ro.key;
    WAIT_L0(); BAR();
    unsigned wbest = s_key[0];
#pragma unroll
    for (int k = 1; k < 8; ++k) { const unsigned v = s_key[k]; wbest = v > wbest ? v : wbest; }
    const bool isbest = (ro.key == wbest);
    if (isbest) {
        if (lane == 0) __hip_atomic_store((unsigned*)(ho + HO_KEY + b * 64), ro.key, RLX_AGENT);
        const u32x4 nv = {nosent(ro.nv[0]), nosent(ro.nv[1]), nosent(ro.nv[2]), nosent(ro.nv[3])};
        __builtin_amdgcn_raw_buffer_store_b128(nv, rs_ho, HO_HN + (myrow >> 3) * 1024 + lane * 16, 0, 16);
    } else __builtin_amdgcn_raw_buffer_store_b128(ro.ov, rs_out, myrow * (D_ * 2) + lane * 16, 0, 16);
    if (!ffn) {
        if (isbest) {
            unsigned win = SENT;
#pragma nounroll
            for (unsigned spins = 0; spins < SPIN_CAP; ++spins) {
                __builtin_amdgcn_s_sleep(8);
                win = __builtin_amdgcn_readfirstlane(__hip_atomic_load((const unsigned*)(ho + HO_WIN + (b & 7) * 256), RLX_AGENT));
                if (win != SENT) break;
            }
            if (win != ro.key) __builtin_amdgcn_raw_buffer_store_b128(ro.ov, rs_out, myrow * (D_ * 2) + lane * 16, 0, 16);
        }
        return;
    }
    f32x2 gf[8][4], uf[8][4];
    if (w >= 4) {
#pragma unroll
        for (int i = 0; i < 8; ++i)
#pragma unroll
            for (int q = 0; q < 4; ++q) {
                gf[i][q] = (f32x2){bflo(g[i][q]), bfhi(g[i][q])};
                uf[i][q] = (f32x2){bflo(u[i][q]), bfhi(u[i][q])};
                asm volatile("" : "+v"(gf[i][q]), "+v"(uf[i][q]));
            }
    } else {
        unsigned best = 0;
        const unsigned* kp = (const unsigned*)(ho + HO_KEY + (w * 64 + lane) * 64);
        __builtin_amdgcn_s_sleep(20);
#pragma nounroll
        for (unsigned spins = 0; spins < SPIN_CAP; ++spins) {
            const unsigned v = __hip_atomic_load(kp, RLX_AGENT);
            if (__all(v != SENT)) { best = wave_umax(v); break; }
            __builtin_amdgcn_s_sleep(1);
        }
        if (lane == 0) s_key[8 + w] = best;
    }
    WAIT_L0(); BAR();
    unsigned best = s_key[8];
#pragma unroll
    for (int k = 1; k < 4; ++k) { const unsigned v = s_key[8 + k]; best = v > best ? v : best; }
    const int row = 2047 - (int)(best & 2047u);
    if (w >= 4) {
        const int lw = w - 4, c = lane & 3, rl = lane >> 2;
        u32x4 nv;
#pragma nounroll
        for (unsigned spins = 0; spins < SPIN_CAP; ++spins) {
            nv = __builtin_amdgcn_raw_buffer_load_b128(rs_ho, HO_HN + (row >> 3) * 1024 + lane * 16, 0, 16);
            if (__all(umax4(nv) != SENT)) break;
            __builtin_amdgcn_s_sleep(1);
        }
        unsigned* myr = s_row + lw * 256;
        *(u32x4*)(myr + lane * 4) = nv;
        f32x2 ag2[4], au2[4];
#pragma unroll
        for (int q = 0; q < 4; ++q) { ag2[q] = (f32x2){0.f, 0.f}; au2[q] = (f32x2){0.f, 0.f}; }
#pragma unroll
        for (int i = 0; i < 8; ++i) {
            const int d = (i * 4 + lw) * 16 + rl;
            const float xd = bf2f(((const unsigned short*)myr)[d]);
#pragma unroll
            for (int q = 0; q < 4; ++q) { ag2[q] += gf[i][q] * (f32x2){xd, xd}; au2[q] += uf[i][q] * (f32x2){xd, xd}; }
        }
        float ag[8], au[8];
#pragma unroll
        for (int q = 0; q < 8; ++q) { ag[q] = ag2[q >> 1][q & 1]; au[q] = au2[q >> 1][q & 1]; }
#pragma unroll
        for (int q = 0; q < 8; ++q) {
            ag[q] = dpp_add<0x124>(ag[q]); ag[q] = dpp_add<0x128>(ag[q]);
            au[q] = dpp_add<0x124>(au[q]); au[q] = dpp_add<0x128>(au[q]);
        }
        if ((lane & 12) == 0) {
            float* rp = s_buf + lw * 528 + (lane >> 4) * 64 + c * 16;
            *(f32x4*)(rp) = (f32x4){ag[0], ag[1], ag[2], ag[3]};
            *(f32x4*)(rp + 4) = (f32x4){ag[4], ag[5], ag[6], ag[7]};
            *(f32x4*)(rp + 8) = (f32x4){au[0], au[1], au[2], au[3]};
            *(f32x4*)(rp + 12) = (f32x4){au[4], au[5], au[6], au[7]};
        }
    } else {
        if (b < 8 && tid == 0) __hip_atomic_store((unsigned*)(ho + HO_WIN + b * 256), best, RLX_AGENT);
#pragma unroll
        for (int i = 0; i < 8; ++i)
#pragma unroll
            for (int q = 0; q < 4; ++q) {
                gf[i][q] = (f32x2){bflo(dn[i][q]), bfhi(dn[i][q])};
                asm volatile("" : "+v"(gf[i][q]));
            }
    }
    if (isbest && ro.key != best) __builtin_amdgcn_raw_buffer_store_b128(ro.ov, rs_out, myrow * (D_ * 2) + lane * 16, 0, 16);
    WAIT_L0(); BAR();
    if (w < 4) {
        float s = 0.f;
#pragma unroll
        for (int k = 0; k < 4; ++k)
#pragma unroll
            for (int r4 = 0; r4 < 4; ++r4) s += s_buf[k * 528 + r4 * 64 + lane];
        const float up = __int_as_float(__builtin_amdgcn_update_dpp(0, __float_as_int(s), 0x128, 0xf, 0xf, true));
        const float gb = rbf(s);
        const float sg = rbf(gb / (1.0f + __expf(-gb)));
        const float hid = rbf(sg * rbf(up));
        f32x2 ay[4];
#pragma unroll
        for (int q = 0; q < 4; ++q) ay[q] = (f32x2){0.f, 0.f};
#pragma unroll
        for (int i = 0; i < 8; ++i) {
            const int f = i * 4 + w;
            const float hv = __int_as_float(__builtin_amdgcn_readlane(__float_as_int(hid), (f >> 3) * 16 + (f & 7)));
#pragma unroll
            for (int q = 0; q < 4; ++q) ay[q] += gf[i][q] * (f32x2){hv, hv};
        }
        *(f32x4*)(s_buf + (4 + w) * 528 + lane * 8) = (f32x4){ay[0][0], ay[0][1], ay[1][0], ay[1][1]};
        *(f32x4*)(s_buf + (4 + w) * 528 + lane * 8 + 4) = (f32x4){ay[2][0], ay[2][1], ay[3][0], ay[3][1]};
    } else if (b < 8 && w == 7) {
        xv = *(const u32x4*)(x + (size_t)row * D_ + lane * 8);
        row_load(prow, rs_ho, row, lane);
    }
    WAIT_L0(); BAR();
    if (tid < 128) {
        const f32x4 y = (*(const f32x4*)(s_buf + 4 * 528 + tid * 4) + *(const f32x4*)(s_buf + 5 * 528 + tid * 4)) +
                        (*(const f32x4*)(s_buf + 6 * 528 + tid * 4) + *(const f32x4*)(s_buf + 7 * 528 + tid * 4));
        const u32x4 yv = {nosent(__float_as_uint(y[0])), nosent(__float_as_uint(y[1])), nosent(__float_as_uint(y[2])), nosent(__float_as_uint(y[3]))};
        __builtin_amdgcn_raw_buffer_store_b128(yv, rs_ho, HO_Y + b * 2048 + tid * 16, 0, 16);
    }
    if (b >= 8) return;
    {
        if (w == 7) {
            const RowOut rw = row_finish(prow, row, xv, wv);
            *(u32x4*)(s_row + 1024 + lane * 4) = rw.nv;
            *(u32x4*)(s_row + 1280 + lane * 4) = rw.hv;
        }
        float s = 0.f;
#pragma nounroll
        for (unsigned spins = 0; spins < SPIN_CAP; ++spins) {
            unsigned v[7];
#pragma unroll
            for (int k = 0; k < 7; ++k) v[k] = __builtin_amdgcn_raw_buffer_load_b32(rs_ho, HO_Y + (w * 7 + k) * 2048 + (b * 64 + lane) * 4, 0, 16);
            s = 0.f; unsigned mx = 0u;
#pragma unroll
            for (int k = 0; k < 7; ++k) { mx = mx > v[k] ? mx : v[k]; s += __uint_as_float(v[k]); }
            if (__all(mx != SENT)) break;
            __builtin_amdgcn_s_sleep(1);
        }
        float* s_fin = s_buf;
        s_fin[w * 64 + lane] = s;
        WAIT_L0(); BAR();
        if (tid < 32) {
            float y0 = 0.f, y1 = 0.f;
#pragma unroll
            for (int k = 0; k < 8; ++k) { y0 += s_fin[k * 64 + 2 * tid]; y1 += s_fin[k * 64 + 2 * tid + 1]; }
            const unsigned ord = (best >> 11) & 0xffffu;
            const unsigned rbits = (ord & 0x8000u) ? (ord & 0x7fffu) : (~ord & 0xffffu);
            const float br = __uint_as_float(rbits << 16);
            const float rs = rbf(1.0f / (1.0f + __expf(-br)));
            const unsigned nw = s_row[1024 + b * 32 + tid], hw = s_row[1280 + b * 32 + tid];
            const float u0 = rbf(rbf(y0) * rs), u1 = rbf(rbf(y1) * rs);
            const float o0 = bflo(hw) + rbf(bflo(nw) + u0), o1 = bfhi(hw) + rbf(bfhi(nw) + u1);
            *(unsigned*)(out + (size_t)row * D_ + b * 64 + 2 * tid) = pack2(o0, o1);
        }
        if (b == 0 && tid == 64) *(unsigned*)(ho + HO_EPOCH) = tag;
    }
}

extern "C" void kernel_launch(void* const* d_in, const int* in_sizes, int n_in, void* d_out, int out_size, void* d_ws,
                              size_t ws_size, hipStream_t stream) {
    char* ws = (char*)d_ws;
    (void)hipFuncSetAttribute((const void*)fused, hipFuncAttributeMaxDynamicSharedMemorySize, LDS_BYTES);
    fused<<<256, 512, LDS_BYTES, stream>>>((const bf16_t*)d_in[0], (const bf16_t*)d_in[1], (const bf16_t*)d_in[2],
                                           (const bf16_t*)d_in[3], (const bf16_t*)d_in[4], (const bf16_t*)d_in[6],
                                           (const bf16_t*)d_in[7], (const bf16_t*)d_in[8], (const bf16_t*)d_in[9],
                                           ws, (bf16_t*)d_out);
}
```
